```python
import torch

def _softmax(x, axis=-1):
    return torch.softmax(x, dim=axis)
def _gelu(x):
    return torch.nn.functional.gelu(x, approximate="tanh")
def _silu(x):
    return torch.nn.functional.silu(x)
def _sigmoid(x):
    return torch.sigmoid(x)
def _layernorm(x, axis=-1, eps=1e-5):
    m = x.mean(dim=axis, keepdim=True)
    v = x.var(dim=axis, keepdim=True, unbiased=False)
    return (x - m) / torch.sqrt(v + eps)
def _scatter_add(tgt, idx, src, axis=0):
    return tgt.clone().index_add_(axis, idx, src)
def _segment_sum(src, seg, n):
    out = torch.zeros((n,) + src.shape[1:], dtype=src.dtype)
    return out.index_add_(0, seg, src)
def _pad(x, pw):
    flat = tuple(v for pair in reversed(pw) for v in pair)
    return torch.nn.functional.pad(x, flat)

kernel_name = 'parallel_sum__linear_attn_block__glu_gated_glu__prompt'

def setup_inputs():

    _w = (lambda a: (lambda t: t.to(torch.bfloat16) if t.is_floating_point() else t)(torch.as_tensor(a)))
    def _A_setup():
        B, S, D = 1, 2048, 640
        return {
            "x": _w((lambda *s: torch.randn(*s, dtype=torch.bfloat16) * 0.05)(B, S, D)),
            "wqkv": _w((lambda *s: torch.randn(*s, dtype=torch.bfloat16) * 0.2)(D, 3 * D)),
            "wo": _w((lambda *s: torch.randn(*s, dtype=torch.bfloat16) * 0.2)(D, D)),
        }
    _A_in = _A_setup()

    def _B_setup():
        B, S, D, F = 1, 2048, 640, 1728
        return {
            "x": _w((lambda *s: torch.randn(*s, dtype=torch.bfloat16) * 0.05)(B, S, D)),
            "w_gate": _w((lambda *s: torch.randn(*s, dtype=torch.bfloat16) * 0.05)(D, F)),
            "w_up": _w((lambda *s: torch.randn(*s, dtype=torch.bfloat16) * 0.05)(D, F)),
            "w_down": _w((lambda *s: torch.randn(*s, dtype=torch.bfloat16) * 0.05)(F, D)),
        }
    _B_in = _B_setup()

    return {
        "A_x": _A_in["x"],
        "A_wqkv": _A_in["wqkv"],
        "A_wo": _A_in["wo"],
        "B_x": _B_in["x"],
        "B_w_gate": _B_in["w_gate"],
        "B_w_up": _B_in["w_up"],
        "B_w_down": _B_in["w_down"],
    }

def reference(A_x, A_wqkv, A_wo, B_x, B_w_gate, B_w_up, B_w_down):
    def _A_ref(x, wqkv, wo):
        B, S, D = x.shape
        H = 32; Dh = D // H
        qkv = (lambda x, *d: x.permute(*d))((x @ wqkv).reshape(B, S, 3, H, Dh), 2, 0, 3, 1, 4)
        q, k, v = qkv[0], qkv[1], qkv[2]
        phi = lambda t: torch.where(t > 0, t + 1, torch.exp(t)) + 1e-6
        pq, pk = phi(q), phi(k)
        kv = torch.einsum("bhsd,bhse->bhde", pk, v)
        z = torch.einsum("bhsd->bhd", pk)
        num = torch.einsum("bhqd,bhde->bhqe", pq, kv)
        den = torch.einsum("bhqd,bhd->bhq", pq, z)[..., None] + 1e-6
        o = num / den
        return (lambda x, *d: x.permute(*d))(o, 0, 2, 1, 3).reshape(B, S, D) @ wo

    def _B_ref(x, w_gate, w_up, w_down):
        g = torch.einsum("bsd,df->bsf", x, w_gate)
        u = torch.einsum("bsd,df->bsf", x, w_up)
        h = _sigmoid(g) * u
        return torch.einsum("bsf,fd->bsd", h, w_down)

    x = A_x
    xn = _layernorm(x, axis=-1)
    return x + _A_ref(xn, A_wqkv, A_wo) + _B_ref(xn, B_w_gate, B_w_up, B_w_down)
```

```cpp
#include <hip/hip_runtime.h>
#include <hip/hip_bf16.h>
typedef unsigned short u16;
__device__ __forceinline__ float bf2f(u16 b){ return __uint_as_float(((unsigned)b)<<16); }
__device__ __forceinline__ u16 f2bf(float f){ unsigned u=__float_as_uint(f); u += 0x7fffu + ((u>>16)&1u); return (u16)(u>>16); }
__device__ __forceinline__ float rbf(float f){ return bf2f(f2bf(f)); }
constexpr int S=2048, D=640, F=1728, H=32, DH=20;

__global__ void ln_k(const u16* x, u16* xn){
  int row=blockIdx.x; int t=threadIdx.x;
  float v[10]; float s=0;
  for(int i=0;i<10;i++){ v[i]=bf2f(x[row*D+t+64*i]); s+=v[i]; }
  for(int o=32;o;o>>=1) s+=__shfl_xor(s,o);
  float m=s/D; float q=0;
  for(int i=0;i<10;i++){ float d=v[i]-m; q+=d*d; }
  for(int o=32;o;o>>=1) q+=__shfl_xor(q,o);
  float var=rbf(q/D); float mb=rbf(m);
  float sd=rbf(sqrtf(rbf(var+1e-5f)));
  for(int i=0;i<10;i++) xn[row*D+t+64*i]=f2bf(rbf(v[i]-mb)/sd);
}
__global__ void gemm_k(const u16* A, const u16* B, float* C, int M,int N,int K, int ldc){
  __shared__ float As[16][65], Bs[16][65];
  int bm=blockIdx.y*64, bn=blockIdx.x*64; int tx=threadIdx.x&15, ty=threadIdx.x>>4;
  float acc[4][4]={};
  for(int k0=0;k0<K;k0+=16){
    for(int i=threadIdx.x;i<1024;i+=256){ int r=i>>4,c=i&15; As[c][r]=bf2f(A[(bm+r)*K+k0+c]); }
    for(int i=threadIdx.x;i<1024;i+=256){ int r=i>>6,c=i&63; Bs[r][c]=bf2f(B[(k0+r)*N+bn+c]); }
    __syncthreads();
    for(int k=0;k<16;k++) for(int i=0;i<4;i++) for(int j=0;j<4;j++) acc[i][j]+=As[k][ty*4+i]*Bs[k][tx*4+j];
    __syncthreads();
  }
  for(int i=0;i<4;i++) for(int j=0;j<4;j++) C[(bm+ty*4+i)*ldc+bn+tx*4+j]=acc[i][j];
}
__device__ __forceinline__ float phi(float t){ return rbf((t>0? rbf(t+1.f): rbf(__expf(t)))+1e-6f); }
__global__ void kv_k(const float* qkv, float* kv, float* z){
  int h=blockIdx.x; int t=threadIdx.x;
  if(t<400){ int d=t/20,e=t%20; float a=0; for(int s=0;s<S;s++) a+=phi(rbf(qkv[s*1920+640+h*20+d]))*rbf(qkv[s*1920+1280+h*20+e]); kv[h*400+t]=rbf(a);}
  else if(t<420){ int d=t-400; float a=0; for(int s=0;s<S;s++) a+=phi(rbf(qkv[s*1920+640+h*20+d])); z[h*20+d]=rbf(a);}
}
__global__ void o_k(const float* qkv, const float* kv, const float* z, u16* o){
  int s=blockIdx.x; int t=threadIdx.x;
  int h=t/20,e=t%20; float num=0,den=0;
  for(int d=0;d<20;d++){ float p=phi(rbf(qkv[s*1920+h*20+d])); num+=p*kv[h*400+d*20+e]; den+=p*z[h*20+d]; }
  den=rbf(rbf(den)+1e-6f); o[s*640+t]=f2bf(rbf(num)/den);
}
__global__ void h_k(const float* g, const float* u, u16* h){
  int i=blockIdx.x*256+threadIdx.x; float gg=rbf(g[i]); float sg=rbf(1.f/(1.f+__expf(-gg))); h[i]=f2bf(sg*rbf(u[i]));
}
__global__ void fin_k(const u16* x, const float* a, const float* b, u16* out){
  int i=blockIdx.x*256+threadIdx.x; out[i]=f2bf(rbf(bf2f(x[i])+rbf(a[i]))+rbf(b[i]));
}
extern "C" void kernel_launch(void* const* d_in, const int* in_sizes, int n_in, void* d_out, int out_size, void* d_ws, size_t ws_size, hipStream_t stream){
  const u16* x=(const u16*)d_in[0]; const u16* wqkv=(const u16*)d_in[1]; const u16* wo=(const u16*)d_in[2];
  const u16* wg=(const u16*)d_in[4]; const u16* wu=(const u16*)d_in[5]; const u16* wd=(const u16*)d_in[6];
  char* ws=(char*)d_ws; size_t off=0;
  u16* xn=(u16*)(ws+off); off+=S*D*2;
  float* qkv=(float*)(ws+off); off+=(size_t)S*1920*4;
  float* g=(float*)(ws+off); off+=(size_t)S*F*4;
  float* u=(float*)(ws+off); off+=(size_t)S*F*4;
  u16* hh=(u16*)(ws+off); off+=(size_t)S*F*2;
  float* kv=(float*)(ws+off); off+=32*400*4;
  float* z=(float*)(ws+off); off+=32*32*4;
  u16* o=(u16*)(ws+off); off+=S*D*2;
  float* a=(float*)(ws+off); off+=S*D*4;
  float* b=(float*)(ws+off); off+=S*D*4;
  ln_k<<<S,64,0,stream>>>(x,xn);
  gemm_k<<<dim3(1920/64,S/64),256,0,stream>>>(xn,wqkv,qkv,S,1920,D,1920);
  gemm_k<<<dim3(F/64,S/64),256,0,stream>>>(xn,wg,g,S,F,D,F);
  gemm_k<<<dim3(F/64,S/64),256,0,stream>>>(xn,wu,u,S,F,D,F);
  kv_k<<<32,448,0,stream>>>(qkv,kv,z);
  o_k<<<S,640,0,stream>>>(qkv,kv,z,o);
  h_k<<<S*F/256,256,0,stream>>>(g,u,hh);
  gemm_k<<<dim3(D/64,S/64),256,0,stream>>>(o,wo,a,S,D,D,D);
  gemm_k<<<dim3(D/64,S/64),256,0,stream>>>(hh,wd,b,S,D,F,D);
  fin_k<<<S*D/256,256,0,stream>>>(x,a,b,(u16*)d_out);
}
```

```cpp
#include <hip/hip_runtime.h>
#include <hip/hip_bf16.h>
#define ACTMODE 0
#define POLL_SLEEP 4
typedef unsigned short u16;
typedef unsigned long long u64;
typedef short s16x4 __attribute__((ext_vector_type(4)));
typedef __bf16 bf16x2 __attribute__((ext_vector_type(2)));
typedef __bf16 bf16x8 __attribute__((ext_vector_type(8)));
typedef float f32x16 __attribute__((ext_vector_type(16)));
typedef float f32x4 __attribute__((ext_vector_type(4)));
typedef unsigned u32x4 __attribute__((ext_vector_type(4)));
typedef unsigned u32x2 __attribute__((ext_vector_type(2)));
#define LAS __attribute__((address_space(3)))
#define GAS __attribute__((address_space(1)))
#define DI __device__ __forceinline__
#define RLX_AGENT __ATOMIC_RELAXED, __HIP_MEMORY_SCOPE_AGENT

constexpr int S = 2048, D = 640, F = 1728;
constexpr int NSL = 94;
constexpr int XS = 0, WS = 49152, LDS_BYTES = 163840;
constexpr int KVI = 16384;
constexpr int SPIN_MAX = 1 << 17;
constexpr unsigned W_EPOCH = 0, W_FLAGH = 256  , W_FLAG1 = 1024  , W_FLAGO = 4096  ,
                   W_ROWST = 16384, W_COLST = W_ROWST + 2048 * 8, W_ACT = 1 << 20  ,
                   W_KVP = 14 << 20  , W_W2F = 16 << 20  , W_ZERO = W_W2F + 74 * 40 * 1024,
                   W_STAMP = 20 << 20;
constexpr int T2 = 122880;

DI float rbf(float f) { return (float)(__bf16)f; }
DI unsigned pk2(float a, float b) { bf16x2 v = {(__bf16)a, (__bf16)b}; return __builtin_bit_cast(unsigned, v); }
DI float lo16(unsigned u) { return __uint_as_float(u << 16); }
DI float hi16(unsigned u) { return __uint_as_float(u & 0xffff0000u); }
DI float dot2(unsigned a, unsigned b, float c) { return __builtin_amdgcn_fdot2_f32_bf16(__builtin_bit_cast(bf16x2, a), __builtin_bit_cast(bf16x2, b), c, false); }
DI f32x16 mfma32(u32x4 a, u32x4 b, f32x16 c) { return __builtin_amdgcn_mfma_f32_32x32x16_bf16(__builtin_bit_cast(bf16x8, a), __builtin_bit_cast(bf16x8, b), c, 0, 0, 0); }
DI f32x4 mfma16(u32x4 a, u32x4 b, f32x4 c) { return __builtin_amdgcn_mfma_f32_16x16x32_bf16(__builtin_bit_cast(bf16x8, a), __builtin_bit_cast(bf16x8, b), c, 0, 0, 0); }
DI void dma16(unsigned ldsaddr, unsigned voff, const char* sbase) {
  asm volatile("s_mov_b32 m0, %0\n\ts_nop 0\n\tglobal_load_lds_dwordx4 %1, %2" ::"s"(ldsaddr), "v"(voff), "s"(sbase));
}
DI void aload(u32x4& d, unsigned voff, const char* sbase) { asm volatile("global_load_dwordx4 %0, %1, %2 sc1" : "=v"(d) : "v"(voff), "s"(sbase)); }
template <int OFF> DI void gload(u32x4& d, unsigned voff, const char* sbase) { asm volatile("global_load_dwordx4 %0, %1, %2 offset:%3" : "=v"(d) : "v"(voff), "s"(sbase), "i"(OFF)); }
DI void aload32(unsigned& d, unsigned voff, const char* sbase) { asm volatile("global_load_dword %0, %1, %2 sc1" : "=v"(d) : "v"(voff), "s"(sbase)); }
DI u32x2 trread(unsigned addr) { return __builtin_bit_cast(u32x2, __builtin_amdgcn_ds_read_tr16_b64_v4i16((LAS s16x4*)addr)); }
DI u32x4 trfrag(unsigned a0, unsigned a1) { const u32x2 lo = trread(a0), hi = trread(a1); return u32x4{lo.x, lo.y, hi.x, hi.y}; }
DI u32x4 ldsread128(unsigned addr) { return *(LAS u32x4*)addr; }
DI void st_gran(char* p, unsigned tag, unsigned v) { __hip_atomic_store((GAS u64*)p, ((u64)tag << 32) | v, RLX_AGENT); }
DI unsigned ld_flag(const char* p) { return __hip_atomic_load((const GAS unsigned*)p, RLX_AGENT); }
DI void st_flag(char* p, unsigned v) { __hip_atomic_store((GAS unsigned*)p, v, RLX_AGENT); }
typedef __amdgpu_buffer_rsrc_t rsrc_t;
DI rsrc_t mk_rsrc(char* ws) { return __builtin_amdgcn_make_buffer_rsrc(ws, 0, 0x7fffffff, 0x00020000); }
DI void st128(rsrc_t rs, unsigned off, u32x4 v) { __builtin_amdgcn_raw_buffer_store_b128(v, rs, off, 0, 16); }
DI void st32(rsrc_t rs, unsigned off, unsigned v) { __builtin_amdgcn_raw_buffer_store_b32(v, rs, off, 0, 16); }
DI u32x4 ld128(rsrc_t rs, unsigned off) { return __builtin_amdgcn_raw_buffer_load_b128(rs, off, 0, 16); }
DI u32x2 ld64(rsrc_t rs, unsigned off) { return __builtin_amdgcn_raw_buffer_load_b64(rs, off, 0, 16); }
DI unsigned ld32(rsrc_t rs, unsigned off) { return __builtin_amdgcn_raw_buffer_load_b32(rs, off, 0, 16); }
DI float exp2_sat(float t) { return __builtin_amdgcn_fmed3f(__builtin_amdgcn_exp2f(t), 0.f, 1.f); }
DI float phi(float a, float rs, float rsl, float ce) { return __builtin_fmaf(fmaxf(a, ce), rs, exp2_sat(a * rsl)); }

#define SB() __builtin_amdgcn_sched_barrier(0)
#define FENCE() asm volatile("" ::: "memory")
#define WCNT(V, L) (((V) & 15) | (7 << 4) | ((L) << 8) | (((V) >> 4) << 14))
template <int N> DI void wait_v() { SB(); FENCE(); __builtin_amdgcn_s_waitcnt(WCNT(N, 15)); FENCE(); SB(); }
template <int N> DI void wait_vl() { SB(); FENCE(); __builtin_amdgcn_s_waitcnt(WCNT(N, 0)); FENCE(); SB(); }
template <int N> DI void wait_bar() { SB(); FENCE(); __builtin_amdgcn_s_waitcnt(WCNT(N, 0)); __builtin_amdgcn_s_barrier(); FENCE(); SB(); }
DI void drain_bar() { SB(); asm volatile("s_waitcnt vmcnt(0) lgkmcnt(0)\n\ts_barrier" ::: "memory"); SB(); }
DI void poll(const char* p, bool act, unsigned tag) {
  for (int spin = 0; spin < SPIN_MAX; ++spin) {
    if (__all(!act || ld_flag(p) == tag)) break;
    __builtin_amdgcn_s_sleep(POLL_SLEEP);
  }
  __builtin_amdgcn_fence(__ATOMIC_ACQUIRE, "wavefront");
}
#define STAMP(i)
#define STAMP_DECL
#define STAMP_OUT
#define STAMP_ARGS
#define STAMP_PASS

DI int w2_row(int ks, int G, int j) {
  if (ks < 54) return 32 * ks + (ks % 3 == 2 ? 8 * G + j : 16 * (j >> 2) + 4 * G + (j & 3));
  const int i = ks - 54;
  if (i < 16) return 20 * (2 * i + (j >> 2)) + 4 * G + (j & 3);
  return 20 * (2 * (4 * (i - 16) + G) + (j >> 2)) + 16 + (j & 3);
}

DI unsigned helper(int hj, const u16* x, const u16* wqkv, const u16* wg, const u16* wu, const u16* wd, const u16* wo, char* ws STAMP_ARGS) {
  const int tid = threadIdx.x, lane = tid & 63, w = __builtin_amdgcn_readfirstlane(tid >> 6);
  const int sub = lane >> 3, ch = lane & 7;
  const rsrc_t rs = mk_rsrc(ws);
  u32x4 xr[2][10];
  {
    const char* xp = (const char*)x + (size_t)(128 * hj + 16 * w) * (D * 2);
    const unsigned vo = sub * (D * 2) + ch * 16;
#define XL(j) gload<j * 128>(xr[0][j], vo, xp); gload<j * 128>(xr[1][j], vo, xp + 8 * D * 2);
    XL(0) XL(1) XL(2) XL(3) XL(4) XL(5) XL(6) XL(7) XL(8) XL(9)
#undef XL
  }
  unsigned tag;
  aload32(tag, W_EPOCH, ws);
  const unsigned reg = w * 20480;
  const int nsl = hj < 4 ? 6 : 5;
  auto slab_dma = [&](int i) {
    const int sl = hj + 16 * i;
    const char* base = sl < 27 ? (const char*)wg + sl * 128 : sl < 54 ? (const char*)wu + (sl - 27) * 128 : (const char*)wqkv + (sl - 54) * 128;
    const int pitch = sl < 54 ? F * 2 : 3 * D * 2;
    const unsigned vo = (80 * w + sub) * pitch + ch * 16;
#pragma unroll
    for (int j = 0; j < 10; ++j) dma16(reg + (i & 1) * 10240 + j * 1024, vo, base + j * 8 * pitch);
  };
  slab_dma(0); slab_dma(1);
  SB();
  asm volatile("s_waitcnt vmcnt(20)" : "+v"(tag)::"memory");
#pragma unroll
  for (int j = 0; j < 20; ++j) asm volatile("" : "+v"(xr[j / 10][j % 10]));
  SB();
  tag += 1u;
#pragma unroll
  for (int b = 0; b < 2; ++b) {
    float s0 = 0.f, q0 = 0.f;
#pragma unroll
    for (int j = 0; j < 10; ++j)
#pragma unroll
      for (int i = 0; i < 4; ++i) { s0 = dot2(xr[b][j][i], 0x3f803f80u, s0); q0 = dot2(xr[b][j][i], xr[b][j][i], q0); }
    s0 += __shfl_xor(s0, 1); q0 += __shfl_xor(q0, 1); s0 += __shfl_xor(s0, 2); q0 += __shfl_xor(q0, 2); s0 += __shfl_xor(s0, 4); q0 += __shfl_xor(q0, 4);
    const float m = s0 * (1.f / D);
    const float sd = sqrtf(rbf(rbf(fmaxf(__builtin_fmaf(-m, m, q0 * (1.f / D)), 0.f)) + 1e-5f));
    if (ch == 0) st_gran(ws + W_ROWST + (128 * hj + 16 * w + 8 * b + sub) * 8, tag, pk2(-m, sd));
  }
  STAMP(1);
  const int hf = lane >> 5, q = (lane >> 2) & 3, p = lane & 3, G = lane >> 4;
  const unsigned ra = reg + (8 * hf + q) * 128 + (G & 1) * 32 + p * 8;
  const u32x4 ones = {0x3f803f80u, 0x3f803f80u, 0x3f803f80u, 0x3f803f80u};
  float cs[6];
#pragma unroll
  for (int i = 0; i < 6; ++i) {
    cs[i] = 0.f;
    if (i < nsl) {
      if (i + 1 < nsl) wait_v<10>(); else wait_v<0>();
      f32x16 c[2];
#pragma unroll
      for (int t = 0; t < 2; ++t) {
#pragma unroll
        for (int e = 0; e < 16; ++e) c[t][e] = 0.f;
#pragma unroll
        for (int ks = 0; ks < 5; ++ks) { const unsigned a = ra + (i & 1) * 10240 + ks * 2048 + t * 64; c[t] = mfma32(ones, trfrag(a, a + 512), c[t]); }
      }
      cs[i] = hf ? c[1][0] : c[0][0];
      wait_vl<63>();
      if (i + 2 < nsl) slab_dma(i + 2);
    }
  }
#pragma unroll
  for (int i = 0; i < 6; ++i) *(LAS float*)(reg + (i * 64 + lane) * 4) = cs[i];
  wait_bar<63>();
  if (w < nsl) {
    float c = 0.f;
#pragma unroll
    for (int j = 0; j < 8; ++j) c += *(LAS float*)(j * 20480 + (w * 64 + lane) * 4);
    const float hi = rbf(c);
    st_gran(ws + W_COLST + ((hj + 16 * w) * 64 + lane) * 8, tag, pk2(hi, c - hi));
  }
  wait_bar<63>();
  STAMP(2);
  auto w2_dma = [&](int un, unsigned dst) {
    const int ks = un / 10, sl = un - ks * 10;
    const char* base = (ks < 54 ? (const char*)wd : (const char*)wo) + sl * 128;
#pragma unroll
    for (int o = 0; o < 4; ++o) {
      const int c = (ch >> 1) ^ (2 * (o & 1) | ((sub >> 1) & 1));
      dma16(dst + o * 1024, w2_row(ks, o, sub) * (D * 2) + c * 32 + (ch & 1) * 16, base);
    }
  };
  auto w2_out = [&](int un, unsigned src) {
    const int ks = un / 10, sl = un - ks * 10;
#pragma unroll
    for (int c = 0; c < 4; ++c) {
      const unsigned a = src + G * 1024 + q * 128 + ((c ^ (2 * (G & 1) | (q >> 1))) * 32) + p * 8;
      st128(rs, W_W2F + (ks * 40 + sl * 4 + c) * 1024 + lane * 16, trfrag(a, a + 512));
    }
  };
  const int gw = hj * 8 + w;
#pragma unroll
  for (int i = 0; i < 5; ++i) w2_dma(gw + 128 * i, reg + i * 4096);
  wait_v<16>();
  w2_out(gw, reg);
  wait_vl<63>();
  const bool six = gw + 640 < 740;
  if (six) w2_dma(gw + 640, reg);
  wait_v<0>();
#pragma unroll
  for (int i = 1; i < 5; ++i) w2_out(gw + 128 * i, reg + i * 4096);
  if (six) w2_out(gw + 640, reg);
  if (hj == 0 && w < 5) st128(rs, W_ZERO + w * 1024 + lane * 16, u32x4{0u, 0u, 0u, 0u});
  drain_bar();
  if (tid == 0) st_flag(ws + W_FLAGH + hj * 4, tag);
  STAMP(3);
  if (w == 0) {
    poll(ws + W_FLAGH + (lane & 15) * 4, true, tag);
    __builtin_amdgcn_fence(__ATOMIC_ACQUIRE, "agent");
  }
  drain_bar();
  return tag;
}

struct Frags { u32x4 x[4]; u32x4 w[2][3]; };
struct P1 {
  const char *xbase, *wbase;
  unsigned voffx, voff[3], wdst, xdst, xa[2], wa[3], rowo, colo[2];
  int wstep;
  rsrc_t rsw;
  u32x2 gr[4], gc[2];
  unsigned epoch, w2f;
};
DI void issueX(const P1& c, int v, int half) { dma16(c.xdst + (v % 3) * 16384 + half * 8192, c.voffx, c.xbase + half * 64 * D * 2 + v * 128); }
DI void issueW1(const P1& c, int j, int pr) { dma16(c.wdst + (j & 3) * 24576 + pr * 4096, c.voff[pr], c.wbase + (size_t)j * c.wstep); }
DI void ld_stat(P1& c, int e) {
  if (e < 4) c.gr[e] = ld64(c.rsw, c.rowo + e * 128);
  else if (e < 6) c.gc[e - 4] = ld64(c.rsw, c.colo[e - 4]);
  else if (e == 6) c.epoch = ld32(c.rsw, W_EPOCH);
  else if (e == 7) c.w2f = ld32(c.rsw, W_FLAGH + (threadIdx.x & 15) * 4);
}
template <int NT> DI void mainloop(P1& c, f32x4 (&acc)[4][2][3] STAMP_ARGS) {
  auto readX = [&](Frags& f, int t, int st) { f.x[st] = ldsread128(c.xa[t & 1] + ((t >> 1) % 3) * 16384 + st * 2048); };
  auto readW = [&](Frags& f, int t, int n) {
    const unsigned a = c.wa[n % NT] + (t & 1) * 24576 + (n / NT) * 12288;
    f.w[n / NT][n % NT] = trfrag(a, a + 512);
  };
  auto issue = [&](int t, int e) {
    if (t == 0) { if (e < 3) issueW1(c, 2, e); else if (e == 3) issueX(c, 1, 1); else if (e < 7) issueW1(c, 3, e - 4); else if (e == 7) issueX(c, 2, 0); }
    else if (t == 1) { if (e < 3) issueW1(c, 4, e); else if (e == 3) issueX(c, 2, 1); else if (e < 7) issueW1(c, 5, e - 4); else if (e == 7) issueX(c, 3, 0); }
    else if (e < 3) { if (t + 4 < 20) issueW1(c, t + 4, e); }
    else if (e == 3) { if ((t + 5) / 2 < 10) issueX(c, (t + 5) / 2, (t + 1) & 1); }
  };
  auto step = [&](int t, const Frags& f, Frags& fn) {
    const bool rd = t + 1 < 20;
    if (rd && ((t + 1) & 1) == 0) {
#pragma unroll
      for (int i = 0; i < NT; ++i) { c.wa[i] = ((t + 1) & 2) ? c.wa[i] + 49152 : c.wa[i] - 49152; asm volatile("" : "+v"(c.wa[i])); }
    }
#pragma unroll
    for (int i = 0; i < 8 * NT; ++i) {
      const int n = i >> 2, st = (n & 1) ? 3 - (i & 3) : (i & 3);
      acc[st][n / NT][n % NT] = mfma16(f.w[n / NT][n % NT], f.x[st], acc[st][n / NT][n % NT]);
      if ((i & 1) == 0) issue(t, i >> 1);
      if (t == 17 && i < 8) ld_stat(c, i);
      if (rd) {
        const int e = NT == 3 ? ((i & 1) ? i >> 1 : -1) : (i < 12 && i % 3 != 2 ? i - i / 3 : -1);
        if (e == 0) readW(fn, t + 1, 0);
        else if (e >= 1 && e <= 4) readX(fn, t + 1, e - 1);
        else if (e >= 5 && e < 4 + 2 * NT) readW(fn, t + 1, e - 4);
      }
      SB();
    }
  };
  Frags f0, f1;
  wait_bar<4>();
  STAMP(1);
#pragma unroll
  for (int i = 0; i < 4; ++i) readX(f0, 0, i);
#pragma unroll
  for (int i = 0; i < 2 * NT; ++i) readW(f0, 0, i);
#define STEP(T, N, FC, FN) wait_bar<N>(); step(T, FC, FN);
  STEP(0, 1, f0, f1) STEP(1, 4, f1, f0) STEP(2, 9, f0, f1) STAMP(7); STEP(3, 8, f1, f0) STEP(4, 8, f0, f1)
  STEP(5, 8, f1, f0) STEP(6, 8, f0, f1) STEP(7, 8, f1, f0) STEP(8, 8, f0, f1) STEP(9, 8, f1, f0)
  STEP(10, 8, f0, f1) STEP(11, 8, f1, f0) STEP(12, 8, f0, f1) STEP(13, 8, f1, f0) STEP(14, 8, f0, f1)
  STEP(15, 8, f1, f0) STEP(16, 7, f0, f1) STEP(17, 0, f1, f0)
#undef STEP
  wait_vl<63>(); step(18, f0, f1);
  wait_vl<63>(); step(19, f1, f0);
}

DI unsigned phase1(int rb, int cg, const u16* x, const u16* wqkv, const u16* wg, const u16* wu, char* ws STAMP_ARGS) {
  const int tid = threadIdx.x, lane = tid & 63, w = __builtin_amdgcn_readfirstlane(tid >> 6);
  const int wn = ((w >> 2) << 1) | (w & 1), wm = (w >> 1) & 1;
  P1 c;
  const int side = w & 1, oct = w >> 1, rho = lane >> 3, pi = lane & 7;
  c.voffx = (8 * w + rho) * (D * 2) + ((pi ^ (4 * (w & 1) + (rho >> 1))) << 4);
  c.xbase = (const char*)x + (size_t)rb * 128 * D * 2;
  c.wdst = WS + (side * 12 + oct) * 1024; c.xdst = XS + w * 1024;
  issueX(c, 0, 0); issueX(c, 0, 1);
  const bool glu = cg < 9, isq = cg >= 13;
  auto col0 = [&](int sd) { return glu ? 192 * cg : isq ? 320 * (cg - 13) + 160 * sd : 640 + 640 * sd + 160 * (cg - 9); };
  c.wbase = glu ? (side ? (const char*)wu : (const char*)wg) : (const char*)wqkv;
  const int pitch = glu ? F * 2 : 3 * D * 2;
  {
    const int cc = (pi >> 1) ^ (2 * (oct & 1) | ((rho >> 1) & 1));
    c.voff[0] = (col0(side) + 16 * cc) * 2 + (8 * oct + rho) * pitch + (pi & 1) * 16;
    c.voff[1] = c.voff[0] + 128;
    c.voff[2] = c.voff[0] + 256 - (!glu && cc >= 2 ? 64 : 0);
  }
  c.wstep = 32 * pitch;
  issueW1(c, 0, 0); issueW1(c, 0, 1); issueW1(c, 0, 2);
  STAMP(5);
  SB(); __builtin_amdgcn_s_barrier(); SB();
  issueW1(c, 1, 0); issueW1(c, 1, 1); issueW1(c, 1, 2); issueX(c, 1, 0);
  const int sr = lane & 15, G = lane >> 4;
  const int lc01 = 32 * (3 * (wn >> 1) + (wn & 1)), lc2 = 32 * (3 * (wn >> 1) + 2) + 16 * (wn & 1);
  c.xa[0] = XS + (64 * wm + sr) * 128 + ((G ^ (sr >> 1)) << 4); c.xa[1] = c.xa[0] ^ 64;
  {
    const int q = (lane >> 2) & 3, p = lane & 3;
    const unsigned b0 = WS + G * 1024 + q * 128 + p * 8, sw = 2 * (G & 1) | (q >> 1);
#pragma unroll
    for (int i = 0; i < 3; ++i) { const int lc = i < 2 ? lc01 + 16 * i : lc2; c.wa[i] = b0 + (lc >> 6) * 4096 + ((((lc >> 4) & 3) ^ sw) << 5); }
  }
  const bool nt3 = glu || w < 4;
  c.rsw = mk_rsrc(ws);
  c.rowo = W_ROWST + (128 * rb + 64 * wm + sr) * 8;
#pragma unroll
  for (int m = 0; m < 2; ++m) {
    const int z = min(4 * m + G, 5), sd = z >= 3, i = sd ? z - 3 : z;
    const int lc = i < 2 ? lc01 + 16 * i : (nt3 ? lc2 : lc01);
    c.colo[m] = W_COLST + ((sd ? col0(1) + (glu ? F : 2 * F) : col0(0) + (glu ? 0 : 2 * F)) + lc + sr) * 8;
  }
  asm volatile("" : "+v"(c.rowo), "+v"(c.colo[0]), "+v"(c.colo[1]));
  f32x4 acc[4][2][3];
#pragma unroll
  for (int i = 0; i < 24; ++i) acc[i / 6][(i / 3) & 1][i % 3] = f32x4{0.f, 0.f, 0.f, 0.f};
  STAMP(6);
  if (nt3) mainloop<3>(c, acc STAMP_PASS); else mainloop<2>(c, acc STAMP_PASS);
  STAMP(2);
  const unsigned tag = c.epoch + 1u;
  for (int spin = 0; spin < SPIN_MAX; ++spin) {
    const bool ok = c.gr[0].y == tag && c.gr[1].y == tag && c.gr[2].y == tag && c.gr[3].y == tag && c.gc[0].y == tag && c.gc[1].y == tag;
    if (__all(ok)) break;
    __builtin_amdgcn_s_sleep(2);
#pragma unroll
    for (int e = 0; e < 6; ++e) ld_stat(c, e);
  }
  float sdv[4], rs[4], rsl[4], ce[4];
#pragma unroll
  for (int st = 0; st < 4; ++st) {
    const unsigned g = c.gr[st].x;
    sdv[st] = hi16(g); rs[st] = __builtin_amdgcn_rcpf(sdv[st]);
    rsl[st] = 1.44269504f * rs[st]; ce[st] = 1e-6f * sdv[st];
    const unsigned mm = (g & 0xffffu) * 0x10001u;
#pragma unroll
    for (int z = 0; z < 6; ++z)
      acc[st][z / 3][z % 3] = mfma16(u32x4{c.gc[z >> 2].x, 0u, 0u, 0u}, u32x4{G == (z & 3) ? mm : 0u, 0u, 0u, 0u}, acc[st][z / 3][z % 3]);
  }
  const bool w2ok = __all(c.w2f == tag);
  if (w == 0 && w2ok) __builtin_amdgcn_fence(__ATOMIC_ACQUIRE, "agent");
  STAMP(9);
  const rsrc_t rsw = c.rsw;
  const unsigned actb = W_ACT + (8 * rb + 4 * wm) * NSL * 1024 + lane * 16;
  const unsigned acts = W_ACT + (8 * rb + 4 * wm + (G & 1)) * NSL * 1024 + ((2 * (wn & 1) + (G >> 1)) * 16 + sr) * 16;
  auto store_split = [&](int pair, int ks, u32x2 a, u32x2 b) {
    const auto r0 = __builtin_amdgcn_permlane16_swap(a.x, b.x, false, false), r1 = __builtin_amdgcn_permlane16_swap(a.y, b.y, false, false);
    st128(rsw, acts + (2 * pair * NSL + ks) * 1024, u32x4{r0[0], r1[0], r0[1], r1[1]});
  };
  auto phi4 = [&](int st, f32x4 v) {
    return u32x2{pk2(phi(v[0], rs[st], rsl[st], ce[st]), phi(v[1], rs[st], rsl[st], ce[st])), pk2(phi(v[2], rs[st], rsl[st], ce[st]), phi(v[3], rs[st], rsl[st], ce[st]))};
  };
  auto glu4 = [&](int st, int i) {
    float h[4];
#pragma unroll
    for (int j = 0; j < 4; ++j) {
      const float e = __builtin_amdgcn_exp2f(acc[st][0][i][j] * -rsl[st]);
      h[j] = acc[st][1][i][j] * __builtin_amdgcn_rcpf(__builtin_fmaf(e, sdv[st], sdv[st]));
    }
    return u32x2{pk2(h[0], h[1]), pk2(h[2], h[3])};
  };
  if (glu) {
#pragma unroll
    for (int pair = 0; pair < 2; ++pair) {
#pragma unroll
      for (int st = 2 * pair; st < 2 * pair + 2; ++st) {
        const u32x2 a = glu4(st, 0), b = glu4(st, 1);
        st128(rsw, actb + (st * NSL + 6 * cg + (lc01 >> 5)) * 1024, u32x4{a.x, a.y, b.x, b.y});
      }
      store_split(pair, 6 * cg + (lc2 >> 5), glu4(2 * pair, 2), glu4(2 * pair + 1, 2));
    }
  } else if (isq) {
    const int sp0 = 54 + 10 * (cg - 13);
#pragma unroll
    for (int sd = 0; sd < 2; ++sd)
#pragma unroll
      for (int pair = 0; pair < 2; ++pair) {
#pragma unroll
        for (int st = 2 * pair; st < 2 * pair + 2; ++st) {
          const u32x2 a = phi4(st, acc[st][sd][0]), b = phi4(st, acc[st][sd][1]);
          st128(rsw, actb + (st * NSL + sp0 + 5 * sd + (lc01 >> 5)) * 1024, u32x4{a.x, a.y, b.x, b.y});
        }
        if (nt3) store_split(pair, sp0 + 5 * sd + 2, phi4(2 * pair, acc[2 * pair][sd][2]), phi4(2 * pair + 1, acc[2 * pair + 1][sd][2]));
      }
  } else {
#pragma unroll
    for (int st = 0; st < 4; ++st) {
      const unsigned rowa = KVI + (64 * wm + 16 * st + sr) * 640;
#pragma unroll
      for (int i = 0; i < 3; ++i) {
        if (i == 2 && !nt3) continue;
        const int u = ((i < 2 ? lc01 + 16 * i : lc2) >> 2) + G;
        const f32x4 v = acc[st][1][i];
        *(LAS u32x2*)(rowa + ((u ^ sr) << 3)) = phi4(st, acc[st][0][i]);
        *(LAS u32x2*)(rowa + (((u + 40) ^ sr) << 3)) = u32x2{pk2(v[0] * rs[st], v[1] * rs[st]), pk2(v[2] * rs[st], v[3] * rs[st])};
      }
    }
    wait_bar<63>();
    const int hf = lane >> 5, q = (lane >> 2) & 3, p = lane & 3;
    const int uk = min(5 * w + 4 * (G & 1) + p, 39);
    unsigned rk[2], rv[2];
#pragma unroll
    for (int t = 0; t < 2; ++t) {
      const int row = 8 * hf + 4 * t + q;
      rk[t] = KVI + row * 640 + ((uk ^ row) << 3); rv[t] = KVI + row * 640 + (((uk + 40) ^ row) << 3);
    }
    const bool one = (lane & 31) >= 20;
    f32x16 kv;
#pragma unroll
    for (int i = 0; i < 16; ++i) kv[i] = 0.f;
#pragma unroll
    for (int ks = 0; ks < 8; ++ks) {
      u32x4 vf = trfrag(rv[0] + ks * 10240, rv[1] + ks * 10240);
#pragma unroll
      for (int i = 0; i < 4; ++i) vf[i] = one ? 0x3f803f80u : vf[i];
      kv = mfma32(trfrag(rk[0] + ks * 10240, rk[1] + ks * 10240), vf, kv);
    }
#pragma unroll
    for (int j = 0; j < 3; ++j)
      if ((lane & 31) <= 20 && (j < 2 || hf == 0)) st128(rsw, W_KVP + ((rb * 32 + 8 * (cg - 9) + w) * 3 + j) * 1024 + lane * 16, __builtin_bit_cast(u32x4, f32x4{kv[4 * j], kv[4 * j + 1], kv[4 * j + 2], kv[4 * j + 3]}));
  }
  STAMP(3);
  if (w == 0 && !w2ok) {
    poll(ws + W_FLAGH + (lane & 15) * 4, true, tag);
    __builtin_amdgcn_fence(__ATOMIC_ACQUIRE, "agent");
  }
  drain_bar();
  if (tid == 0) st_flag(ws + W_FLAG1 + rb * 128 + cg * 4, tag);
  return tag;
}

constexpr int AD = 5, JO = 5;
constexpr int a_iter(int n) { return n < AD ? -1 : (n >= 7 && n - AD + 1 < JO) ? JO : n - AD + 1; }
constexpr int p2_wait(int J) {
  int cnt = 0, need = 0;
  for (int n = 0; n < AD; ++n) { cnt += 4; if (n == J) need = cnt; }
  for (int it = 0; it < J; ++it) {
    if (it == JO - 1) { cnt += 1; if (J == JO) need = cnt; }
    if (it + 3 < 10) { cnt += 5; if (it + 3 == J + 1 && cnt > need) need = cnt; }
    for (int n = AD; n < 10; ++n) if (a_iter(n) == it) { cnt += 4; if (n == J && cnt > need) need = cnt; }
  }
  return cnt - need;
}
DI void phase2(int rb2, int cq, int rbo, int P, unsigned tag, const u16* x, u16* out, char* ws STAMP_ARGS) {
  const int tid = threadIdx.x, lane = tid & 63, w = __builtin_amdgcn_readfirstlane(tid >> 6);
  const int kq = w & 3, sh = w >> 2, sr = lane & 15, G = lane >> 4;
  const rsrc_t rsw = mk_rsrc(ws);
  const unsigned l16 = lane * 16;
  const int rot = (7 * (rb2 & 3)) >> 1;
  auto hrow = [&](int i) { const int r = i + rot; return 4 * (r >= 14 ? r - 14 : r) + kq; };
  auto issueW1 = [&](int J, int n) {
    const int i = min(2 * J + sh, 18), ks = i < 14 ? hrow(i) : 54 + 4 * (i - 14) + kq;
    const char* src = ws + ((i < 14 && ks >= 54) ? W_ZERO : W_W2F + (ks * 40 + 5 * cq) * 1024);
    dma16((J % 3) * 40960 + (sh * 4 + kq) * 5120 + n * 1024, l16, src + n * 1024);
  };
  if (w == 0) poll(lane < 16 ? ws + W_FLAG1 + lane * 128 + (9 + (P >> 2)) * 4 : ws + W_FLAG1 + rbo * 128 + (13 + (P >> 3)) * 4, lane < 17, tag);
  wait_bar<63>();
  STAMP(10);
  const int s0 = (40 * P) / 32;
  const unsigned tile = W_ACT + (8 * rbo + w) * NSL * 1024;
  u32x4 pqf[2], part[16];
  aload(pqf[0], l16, ws + tile + (54 + s0) * 1024); aload(pqf[1], l16, ws + tile + (55 + s0) * 1024);
  const int hh = w >= 3, pj = hh ? w - 3 : w;
  const bool pact = w < 6 && (lane & 31) <= 20 && (pj < 2 || lane < 32);
  if (pact) {
#pragma unroll
    for (int i = 0; i < 16; ++i) aload(part[i], l16, ws + W_KVP + ((i * 32 + 2 * P + hh) * 3 + pj) * 1024);
  }
#pragma unroll
  for (int J = 0; J < 3; ++J)
#pragma unroll
    for (int n = 0; n < 5; ++n) issueW1(J, n);
  u32x2 xres[3];
  const int row0 = 64 * rb2 + 32 * sh + sr;
#pragma unroll
  for (int i = 0; i < 3; ++i) {
    const int tl = min(kq + 4 * i, 9);
    xres[i] = *(const u32x2*)(x + (size_t)(row0 + 16 * (tl / 5)) * D + 80 * cq + 16 * (tl % 5) + 4 * G);
  }
  SB();
  asm volatile("s_waitcnt vmcnt(18)" : "+v"(pqf[0]), "+v"(pqf[1])::"memory");
  SB();
  if (pact) {
#pragma unroll
    for (int i = 0; i < 16; ++i) asm volatile("" : "+v"(part[i]));
    f32x4 t = __builtin_bit_cast(f32x4, part[0]);
#pragma unroll
    for (int i = 1; i < 16; ++i) t += __builtin_bit_cast(f32x4, part[i]);
    *(LAS u32x2*)(T2 + hh * 1024 + (lane & 31) * 48 + (8 * pj + 4 * (lane >> 5)) * 2) = u32x2{pk2(t[0], t[1]), pk2(t[2], t[3])};
  }
  if (tid == 448) *(LAS u32x2*)(T2 + 2048) = u32x2{0u, 0u};
  wait_bar<63>();
  {
    u32x4 a[8];
#pragma unroll
    for (int i = 0; i < 8; ++i) {
      const int b = i >> 2, sp = s0 + ((i >> 1) & 1);
      const int e = (i & 1) == 0 ? sr : (sr & 3) == b ? 20 : ((sr & 2) && (sr >> 3) == b) ? 16 + 2 * ((sr >> 2) & 1) + (sr & 1) : -1;
      u32x2 v[2];
#pragma unroll
      for (int hl = 0; hl < 2; ++hl) {
        const int d = 32 * sp + (sp % 5 == 2 ? 8 * G + 4 * hl : 16 * hl + 4 * G) - 20 * (2 * P + b);
        v[hl] = *(LAS u32x2*)((e >= 0 && d >= 0 && d < 20) ? T2 + b * 1024 + e * 48 + d * 2 : T2 + 2048);
      }
      a[i] = u32x4{v[0].x, v[0].y, v[1].x, v[1].y};
    }
    const f32x4 z = {0.f, 0.f, 0.f, 0.f};
    const f32x4 na = mfma16(a[2], pqf[1], mfma16(a[0], pqf[0], z)), nb = mfma16(a[6], pqf[1], mfma16(a[4], pqf[0], z));
    const f32x4 xx = mfma16(a[7], pqf[1], mfma16(a[5], pqf[0], mfma16(a[3], pqf[1], mfma16(a[1], pqf[0], z))));
    const float ra = __builtin_amdgcn_rcpf(xx[0] + 1e-6f), rb = __builtin_amdgcn_rcpf(xx[1] + 1e-6f), rl = G < 2 ? ra : rb;
    st128(rsw, tile + (74 + P) * 1024 + l16, u32x4{pk2(na[0] * ra, na[1] * ra), pk2(na[2] * ra, na[3] * ra), pk2(nb[0] * rb, nb[1] * rb), pk2(nb[2] * rb, nb[3] * rb)});
    st32(rsw, tile + (90 + (P >> 2)) * 1024 + ((P & 3) * 16 + sr) * 16 + 4 * G, pk2(xx[2] * rl, xx[3] * rl));
  }
  STAMP(8);
  if (w == 0) {
    poll(ws + W_FLAG1 + (rb2 >> 1) * 128 + (lane & 15) * 4, (lane & 15) < 9, tag);
  }
  wait_bar<63>();
  STAMP(11);
  const char* abase = ws + W_ACT + (4 * rb2 + 2 * sh) * NSL * 1024;
  u32x4 a[AD][4];
  auto issueA1 = [&](int J, int e) {
    const int i = min(2 * J + (e >> 1), 18), ks = i < 14 ? hrow(i) : 74 + 4 * (i - 14) + kq;
    aload(a[J % AD][e], l16, abase + (((i < 14 && ks >= 54) ? kq : ks) + (e & 1) * NSL) * 1024);
  };
#pragma unroll
  for (int J = 0; J < AD; ++J)
#pragma unroll
    for (int e = 0; e < 4; ++e) issueA1(J, e);
  wait_bar<4 * AD>();
  if (tid == 0) st_flag(ws + W_FLAGO + rbo * 128 + P * 4, tag);
  u32x4 wf[2][10];
  const unsigned wra = kq * 5120 + l16;
  auto readW = [&](int J, int e) { wf[J & 1][e] = ldsread128(wra + (J % 3) * 40960 + (e / 5) * 20480 + (e % 5) * 1024); };
#pragma unroll
  for (int e = 0; e < 10; ++e) readW(0, e);
  f32x4 acc[2][5];
#pragma unroll
  for (int i = 0; i < 10; ++i) acc[i / 5][i % 5] = f32x4{0.f, 0.f, 0.f, 0.f};
  unsigned oflag;
  auto vm = [&](int J, int e) {
    if (J == JO - 1) { if (e == 0) { aload32(oflag, W_FLAGO + (rb2 >> 1) * 128 + sr * 4, ws); return; } --e; }
    if (J + 3 < 10) { if (e < 5) { issueW1(J + 3, e); return; } e -= 5; }
#pragma unroll
    for (int n = AD; n < 10; ++n)
      if (a_iter(n) == J) { if (e >= 0 && e < 4) issueA1(n, e); e -= 4; }
  };
  auto iter = [&](int J) {
    if (J == JO) {
      asm volatile("" : "+v"(oflag));
      if (!__all(oflag == tag)) poll(ws + W_FLAGO + (rb2 >> 1) * 128 + sr * 4, true, tag);
      SB();
    }
#pragma unroll
    for (int i = 0; i < 20; ++i) {
      const int r = i / 10, n = (i % 10) >> 1, st = (i ^ n) & 1;
      if (2 * J + r <= 18) acc[st][n] = mfma16(wf[J & 1][5 * r + n], a[J % AD][2 * r + st], acc[st][n]);
      vm(J, i);
      if (J + 1 < 10 && (i & 1) == 0) readW(J + 1, i >> 1);
      SB();
    }
  };
#define ITER(J)                                                                                                                                      \
  SB();                                                                                                                                              \
  asm volatile("s_waitcnt vmcnt(%4) lgkmcnt(0)\n\ts_barrier" : "+v"(a[J % AD][0]), "+v"(a[J % AD][1]), "+v"(a[J % AD][2]), "+v"(a[J % AD][3]) : "i"(p2_wait(J)) : "memory"); \
  SB();                                                                                                                                              \
  iter(J);
  ITER(0) ITER(1) ITER(2) ITER(3) ITER(4) ITER(5) ITER(6) STAMP(12); ITER(7) ITER(8) ITER(9)
#undef ITER
  STAMP(13);
  wait_bar<63>();
#pragma unroll
  for (int tl = 0; tl < 10; ++tl)
    if ((tl & 3) != kq) *(LAS f32x4*)(((sh * 10 + tl) * 4 + kq) * 1024 + l16) = acc[tl / 5][tl % 5];
  wait_bar<63>();
#pragma unroll
  for (int i = 0; i < 3; ++i) {
    if (i == 2 && kq >= 2) break;
    const int tl = kq + 4 * i;
    f32x4 v = kq == 0 ? acc[(4 * i) / 5][(4 * i) % 5] : kq == 1 ? acc[(4 * i + 1) / 5][(4 * i + 1) % 5]
              : kq == 2 ? acc[((4 * i + 2) % 10) / 5][(4 * i + 2) % 5] : acc[((4 * i + 3) % 10) / 5][(4 * i + 3) % 5];
#pragma unroll
    for (int j = 1; j < 4; ++j) v += *(LAS f32x4*)(((sh * 10 + tl) * 4 + ((kq + j) & 3)) * 1024 + l16);
    const u32x2 o = {pk2(v[0] + lo16(xres[i].x), v[1] + hi16(xres[i].x)), pk2(v[2] + lo16(xres[i].y), v[3] + hi16(xres[i].y))};
    *(u32x2*)(out + (size_t)(row0 + 16 * (tl / 5)) * D + 80 * cq + 16 * (tl % 5) + 4 * G) = o;
  }
}

__global__ __launch_bounds__(512, 2) void fused(const u16* __restrict__ x, const u16* __restrict__ wqkv, const u16* __restrict__ wg,
                                                const u16* __restrict__ wu, const u16* __restrict__ wd, const u16* __restrict__ wo,
                                                u16* __restrict__ out, char* __restrict__ ws) {
  const int xcd = blockIdx.x & 7, idx = blockIdx.x >> 3;
  STAMP_DECL
  STAMP(0);
  unsigned tag;
  if ((xcd & 1) && idx >= 28) tag = helper((xcd >> 1) * 4 + idx - 28, x, wqkv, wg, wu, wd, wo, ws STAMP_PASS);
  else {
    const int i = (xcd & 1) ? idx / 7 : idx >> 3, j = (xcd & 1) ? idx % 7 : idx & 7;
    const int cg = (xcd & 1) ? (j < 4 ? 5 + j : j < 6 ? 7 + j : 14) : (j < 5 ? j : j < 7 ? 4 + j : 13);
    tag = phase1(4 * (xcd >> 1) + i, cg, x, wqkv, wg, wu, ws STAMP_PASS);
  }
  STAMP(4);
  phase2(4 * xcd + (idx >> 3), idx & 7, idx & 15, 2 * xcd + (idx >> 4), tag, x, out, ws STAMP_PASS);
  if (blockIdx.x == 255 && threadIdx.x >= 448) {
    const int lane = threadIdx.x & 63;
#pragma unroll
    for (int i = 0; i < 4; ++i) poll(ws + W_FLAG1 + (4 * i + (lane >> 4)) * 128 + (lane & 15) * 4, (lane & 15) < 15, tag);
    poll(ws + W_FLAGH + (lane & 15) * 4, true, tag);
    if (lane == 0) st_flag(ws + W_EPOCH, tag);
  }
  STAMP(14);
  STAMP_OUT
}

extern "C" void kernel_launch(void* const* d_in, const int* in_sizes, int n_in, void* d_out, int out_size, void* d_ws, size_t ws_size,
                              hipStream_t stream) {
  const u16* x = (const u16*)d_in[0]; const u16* wqkv = (const u16*)d_in[1]; const u16* wo = (const u16*)d_in[2];
  const u16* wg = (const u16*)d_in[4]; const u16* wu = (const u16*)d_in[5]; const u16* wd = (const u16*)d_in[6];
  (void)hipFuncSetAttribute((const void*)fused, hipFuncAttributeMaxDynamicSharedMemorySize, LDS_BYTES);
  fused<<<256, 512, LDS_BYTES, stream>>>(x, wqkv, wg, wu, wd, wo, (u16*)d_out, (char*)d_ws);
}
```

```cpp
#include <hip/hip_runtime.h>
#include <hip/hip_bf16.h>
#define PF_EARLY 16
#define PF_LOOP 10
#define SPREAD 2
#define JOBABL 0
#define ACTMODE 0
#define POLL_SLEEP 4
typedef unsigned short u16;
typedef unsigned long long u64;
typedef short s16x4 __attribute__((ext_vector_type(4)));
typedef __bf16 bf16x2 __attribute__((ext_vector_type(2)));
typedef __bf16 bf16x8 __attribute__((ext_vector_type(8)));
typedef float f32x16 __attribute__((ext_vector_type(16)));
typedef float f32x4 __attribute__((ext_vector_type(4)));
typedef unsigned u32x4 __attribute__((ext_vector_type(4)));
typedef unsigned u32x2 __attribute__((ext_vector_type(2)));
#define LAS __attribute__((address_space(3)))
#define GAS __attribute__((address_space(1)))
#define DI __device__ __forceinline__
#define RLX_AGENT __ATOMIC_RELAXED, __HIP_MEMORY_SCOPE_AGENT

constexpr int S = 2048, D = 640, F = 1728;
constexpr int NSL = 94;
constexpr int XS = 0, WS = 49152, LDS_BYTES = 163840;
constexpr int KVI = 16384;
constexpr int SPIN_MAX = 1 << 14;
constexpr unsigned W_EPOCH = 0, W_FLAG1 = 1024  , W_FLAGO = 4096  ,
                   W_FLAGKV = 8192  ,
                   W_ROWST = 16384, W_COLST = W_ROWST + 2048 * 8, W_ACT = 1 << 20  ,
                   W_KVP = 14 << 20  , W_STAMP = 20 << 20;
constexpr int SPARE = 147456;
constexpr int EAR = 16384, LAT = 98304;
constexpr int T2 = 0;

DI float rbf(float f) { return (float)(__bf16)f; }
DI unsigned pk2(float a, float b) { bf16x2 v = {(__bf16)a, (__bf16)b}; return __builtin_bit_cast(unsigned, v); }
DI float lo16(unsigned u) { return __uint_as_float(u << 16); }
DI float hi16(unsigned u) { return __uint_as_float(u & 0xffff0000u); }
DI float dot2(unsigned a, unsigned b, float c) { return __builtin_amdgcn_fdot2_f32_bf16(__builtin_bit_cast(bf16x2, a), __builtin_bit_cast(bf16x2, b), c, false); }
DI f32x16 mfma32(u32x4 a, u32x4 b, f32x16 c) { return __builtin_amdgcn_mfma_f32_32x32x16_bf16(__builtin_bit_cast(bf16x8, a), __builtin_bit_cast(bf16x8, b), c, 0, 0, 0); }
DI f32x4 mfma16(u32x4 a, u32x4 b, f32x4 c) { return __builtin_amdgcn_mfma_f32_16x16x32_bf16(__builtin_bit_cast(bf16x8, a), __builtin_bit_cast(bf16x8, b), c, 0, 0, 0); }
DI void dma16(unsigned ldsaddr, unsigned voff, const char* sbase) {
  asm volatile("s_mov_b32 m0, %0\n\ts_nop 0\n\tglobal_load_lds_dwordx4 %1, %2" ::"s"(ldsaddr), "v"(voff), "s"(sbase));
}
DI void aload(u32x4& d, unsigned voff, const char* sbase) { asm volatile("global_load_dwordx4 %0, %1, %2 sc1" : "=v"(d) : "v"(voff), "s"(sbase)); }
DI void gload32(unsigned& d, unsigned voff, const char* sbase) { asm volatile("global_load_dword %0, %1, %2" : "=v"(d) : "v"(voff), "s"(sbase)); }
template <int OFF> DI void gload(u32x4& d, unsigned voff, const char* sbase) { asm volatile("global_load_dwordx4 %0, %1, %2 offset:%3" : "=v"(d) : "v"(voff), "s"(sbase), "i"(OFF)); }
DI void aload32(unsigned& d, unsigned voff, const char* sbase) { asm volatile("global_load_dword %0, %1, %2 sc1" : "=v"(d) : "v"(voff), "s"(sbase)); }
DI void sload_epoch(unsigned& e, const char* ws) { asm volatile("s_load_dword %0, %1, 0x0\n\ts_waitcnt lgkmcnt(0)" : "=s"(e) : "s"(ws)); }
DI u32x2 trread(unsigned addr) { return __builtin_bit_cast(u32x2, __builtin_amdgcn_ds_read_tr16_b64_v4i16((LAS s16x4*)addr)); }
DI u32x4 trfrag(unsigned a0, unsigned a1) { const u32x2 lo = trread(a0), hi = trread(a1); return u32x4{lo.x, lo.y, hi.x, hi.y}; }
DI u32x4 ldsread128(unsigned addr) { return *(LAS u32x4*)addr; }
DI void st_gran(char* p, unsigned tag, unsigned v) { __hip_atomic_store((GAS u64*)p, ((u64)tag << 32) | v, RLX_AGENT); }
DI unsigned ld_flag(const char* p) { return __hip_atomic_load((const GAS unsigned*)p, RLX_AGENT); }
DI void st_flag(char* p, unsigned v) { __hip_atomic_store((GAS unsigned*)p, v, RLX_AGENT); }
typedef __amdgpu_buffer_rsrc_t rsrc_t;
DI rsrc_t mk_rsrc(char* ws) { return __builtin_amdgcn_make_buffer_rsrc(ws, 0, 0x7fffffff, 0x00020000); }
DI void st128(rsrc_t rs, unsigned off, u32x4 v) { __builtin_amdgcn_raw_buffer_store_b128(v, rs, off, 0, 16); }
DI void st32(rsrc_t rs, unsigned off, unsigned v) { __builtin_amdgcn_raw_buffer_store_b32(v, rs, off, 0, 16); }
DI u32x4 ld128(rsrc_t rs, unsigned off) { return __builtin_amdgcn_raw_buffer_load_b128(rs, off, 0, 16); }
DI u32x4 ld128s(rsrc_t rs, unsigned voff, unsigned soff) { return __builtin_amdgcn_raw_buffer_load_b128(rs, voff, soff, 16); }
DI u32x2 ld64(rsrc_t rs, unsigned off) { return __builtin_amdgcn_raw_buffer_load_b64(rs, off, 0, 16); }
DI unsigned ld32(rsrc_t rs, unsigned off) { return __builtin_amdgcn_raw_buffer_load_b32(rs, off, 0, 16); }
DI float exp2_sat(float t) { return __builtin_amdgcn_fmed3f(__builtin_amdgcn_exp2f(t), 0.f, 1.f); }
DI float phi(float a, float rs, float rsl, float ce) { return __builtin_fmaf(fmaxf(a, ce), rs, exp2_sat(a * rsl)); }

#define SB() __builtin_amdgcn_sched_barrier(0)
#define FENCE() asm volatile("" ::: "memory")
#define WCNT(V, L) (((V) & 15) | (7 << 4) | ((L) << 8) | (((V) >> 4) << 14))
template <int N> DI void wait_v() { SB(); FENCE(); __builtin_amdgcn_s_waitcnt(WCNT(N, 15)); FENCE(); SB(); }
template <int N> DI void wait_vl() { SB(); FENCE(); __builtin_amdgcn_s_waitcnt(WCNT(N, 0)); FENCE(); SB(); }
template <int N> DI void wait_bar() { SB(); FENCE(); __builtin_amdgcn_s_waitcnt(WCNT(N, 0)); __builtin_amdgcn_s_barrier(); FENCE(); SB(); }
DI void drain_bar() { SB(); asm volatile("s_waitcnt vmcnt(0) lgkmcnt(0)\n\ts_barrier" ::: "memory"); SB(); }
DI void poll(const char* p, bool act, unsigned tag) {
#pragma unroll 1
  for (int spin = 0; spin < SPIN_MAX; ++spin) {
    if (__all(!act || ld_flag(p) == tag)) break;
    __builtin_amdgcn_s_sleep(POLL_SLEEP);
  }
  __builtin_amdgcn_fence(__ATOMIC_ACQUIRE, "wavefront");
}
#define STAMP(i)
#define STAMP_DECL
#define STAMP_OUT
#define STAMP_ARGS
#define STAMP_PASS

template <int CTRL, int RM> DI float dpp_add(float v) {
  return v + __builtin_bit_cast(float, __builtin_amdgcn_update_dpp(0, __builtin_bit_cast(int, v), CTRL, RM, 0xf, true));
}
DI float wave_sum(float v) {
  v = dpp_add<0x111, 0xf>(v); v = dpp_add<0x112, 0xf>(v); v = dpp_add<0x114, 0xf>(v); v = dpp_add<0x118, 0xf>(v);
  v = dpp_add<0x142, 0xa>(v); return dpp_add<0x143, 0xc>(v);
}
DI void row_sums(const u32x4 (&xr)[2], float& s, float& q) {
  s = 0.f; q = 0.f;
#pragma unroll
  for (int i = 0; i < 8; ++i) { s = dot2(xr[i >> 2][i & 3], 0x3f803f80u, s); q = dot2(xr[i >> 2][i & 3], xr[i >> 2][i & 3], q); }
  const bool on = (threadIdx.x & 63) < 40;
  s = wave_sum(on ? s : 0.f); q = wave_sum(on ? q : 0.f);
}
DI unsigned row_stat(float s, float q) {
  const float m = s * (1.f / D);
  return pk2(-m, sqrtf(rbf(rbf(fmaxf(__builtin_fmaf(-m, m, q * (1.f / D)), 0.f)) + 1e-5f)));
}
DI unsigned helper(int half, int hr, const u16* x, const u16* wqkv, const u16* wg, const u16* wu, char* ws STAMP_ARGS) {
  const int tid = threadIdx.x, lane = tid & 63, w = __builtin_amdgcn_readfirstlane(tid >> 6);
  const int sub = lane >> 3, ch = lane & 7;
  unsigned tag;
  sload_epoch(tag, ws);
  {
    u32x4 xr[2];
    const char* xp = (const char*)x + (size_t)(8 * blockIdx.x + w) * (D * 2) + min(lane, 39) * 32;
    xr[0] = *(const GAS u32x4*)xp; xr[1] = *(const GAS u32x4*)(xp + 16);
    float s, q;
    row_sums(xr, s, q);
    if (lane == 63) st_gran(ws + W_ROWST + (8 * blockIdx.x + w) * 8, tag + 1u, row_stat(s, q));
    wait_v<0>();
  }
  if (w < 6) {
    const int n = hr + 8 * w, ng = 14 - half, m = n - 2 * ng;
    if (m < 15) {
      const int sl = n < ng ? 14 * half + n : m < 0 ? 27 + 14 * half + n - ng
                   : m < 5 ? 54 + (half ? (m < 2 ? 3 + m : 5 + m) : (m < 3 ? m : 2 + m)) : m < 10 ? 59 + 5 * half + m : 64 + 5 * half + m;
      const char* base = sl < 27 ? (const char*)wg + sl * 128 : sl < 54 ? (const char*)wu + (sl - 27) * 128 : (const char*)wqkv + (sl - 54) * 128;
      const int pitch = sl < 54 ? F * 2 : 3 * D * 2;
      const unsigned reg = w * 24576, vo = sub * pitch + ch * 16;
      auto chunk = [&](int i) {
#pragma unroll
        for (int j = 0; j < 8; ++j) dma16(reg + (i % 3) * 8192 + j * 1024, vo, base + (size_t)((9 - i) * 64 + j * 8) * pitch);
      };
      chunk(0); chunk(1); chunk(2);
      const unsigned ra = reg + (8 * (lane >> 5) + ((lane >> 2) & 3)) * 128 + ((lane >> 4) & 1) * 32 + (lane & 3) * 8;
      const u32x4 ones = {0x3f803f80u, 0x3f803f80u, 0x3f803f80u, 0x3f803f80u};
      f32x16 c[2];
#pragma unroll
      for (int e = 0; e < 16; ++e) { c[0][e] = 0.f; c[1][e] = 0.f; }
#pragma unroll
      for (int i = 0; i < 10; ++i) {
        if (i < 8) wait_v<16>(); else if (i == 8) wait_v<8>(); else wait_v<0>();
#pragma unroll
        for (int ks = 0; ks < 4; ++ks)
#pragma unroll
          for (int t = 0; t < 2; ++t) { const unsigned a = ra + (i % 3) * 8192 + ks * 2048 + t * 64; c[t] = mfma32(ones, trfrag(a, a + 512), c[t]); }
        wait_vl<63>();
        if (i + 3 < 10) chunk(i + 3);
      }
      const float cs = lane >> 5 ? c[1][0] : c[0][0], hi = rbf(cs);
      st_gran(ws + W_COLST + (sl * 64 + lane) * 8, tag + 1u, pk2(hi, cs - hi));
    }
  }
  STAMP(2);
  return tag + 1u;
}

#define MSTAMP(i)
#define XSTAMP(i) STAMP(i)
#define OSTAMP(i)
#define P2STAMP(J)
#define XV(v) v
struct Frags { u32x4 x[4]; u32x4 w[2][3]; };
struct P1 {
  const char *xbase, *wbase, *xall, *wd;
  char* ws;
  unsigned voffx, voff, wdst, xdst, xa[2], wa[3], rowo, colo[2], tag, epoch;
  int wstep, w, cgp, rb, cb0, cbd, lc01, lc2;
  rsrc_t rsw;
  u32x2 gr[4], gc[2];
  u32x4 xrow[2];
  float rsum, rsq;
  bool early;
};
DI int slot_o(int w, int s) { const int nh = w < 6 ? 7 : 6; return s < nh ? -1 : min((w < 4 ? 2 * w : 8 + 3 * (w - 4)) + s - nh, 19); }
DI void w2_stage(unsigned dst, const char* src, int hs, int gs, int o, int lane) {
  const int rho = lane >> 3, pi = lane & 7;
  const unsigned v = (hs * (rho >> 2) + (rho & 3)) * (D * 2) + (((pi >> 1) ^ ((rho >> 1) & 1)) << 5) + (pi & 1) * 16;
  dma16(dst + o * 1024, v ^ ((o & 1) << 6), src + o * gs * (D * 2));
}
DI void issueX(const P1& c, int v, int half) { dma16(c.xdst + (v % 3) * 16384 + half * 8192, c.voffx, c.xbase + half * 64 * D * 2 + XV(v) * 128); }
DI void issueW1(const P1& c, int j, int pr) { dma16(c.wdst + (j & 3) * 24576 + pr * 4096, c.voff, c.wbase + (size_t)j * c.wstep + pr * 128); }
DI void ld_stat(P1& c, int e) {
  if (e < 4) c.gr[e] = ld64(c.rsw, c.rowo + e * 128);
  else if (e < 6) c.gc[e - 4] = ld64(c.rsw, c.colo[e - 4]);
}
template <int NT> DI void late_setup(P1& c) {
  const int lane = threadIdx.x & 63, sr = lane & 15, G = lane >> 4;
  c.rowo = W_ROWST + (128 * c.rb + 64 * ((threadIdx.x >> 7) & 1) + sr) * 8;
#pragma unroll
  for (int m = 0; m < 2; ++m) {
    const int z = min(4 * m + G, 5), sd = z >= 3, i = sd ? z - 3 : z;
    const int l1 = c.lc01, l2 = NT == 3 ? c.lc2 : l1, lc = i < 2 ? l1 + 16 * i : l2;
    c.colo[m] = W_COLST + (c.cb0 + (sd ? c.cbd : 0) + lc + sr) * 8;
  }
  asm volatile("" : "+v"(c.rowo), "+v"(c.colo[0]), "+v"(c.colo[1]));
}
template <int NT> DI void mainloop(P1& c, f32x4 (&acc)[4][2][3] STAMP_ARGS) {
  auto readX = [&](Frags& f, int t, int st) { f.x[st] = ldsread128(c.xa[t & 1] + ((t >> 1) % 3) * 16384 + st * 2048); };
  auto readW = [&](Frags& f, int t, int n) {
    const unsigned a = c.wa[n % NT] + (t & 1) * 24576 + (n / NT) * 12288;
    f.w[n / NT][n % NT] = trfrag(a, a + 512);
  };
  auto issue = [&](int t, int e) {
    if (t == 0) { if (e < 3) issueW1(c, 2, e); else if (e == 3) issueX(c, 1, 1); else if (e < 7) issueW1(c, 3, e - 4); else if (e == 7) issueX(c, 2, 0); }
    else if (t == 1) { if (e < 3) issueW1(c, 4, e); else if (e == 3) issueX(c, 2, 1); else if (e < 7) issueW1(c, 5, e - 4); else if (e == 7) issueX(c, 3, 0); }
    else if (e < 3) { if (t + 4 < 20) issueW1(c, t + 4, e); }
    else if (e == 3) { if ((t + 5) / 2 < 10) issueX(c, (t + 5) / 2, (t + 1) & 1); }
  };
  auto step = [&](int t, const Frags& f, Frags& fn) {
    const bool rd = t + 1 < 20;
    if (rd && ((t + 1) & 1) == 0) {
#pragma unroll
      for (int i = 0; i < NT; ++i) { c.wa[i] = ((t + 1) & 2) ? c.wa[i] + 49152 : c.wa[i] - 49152; asm volatile("" : "+v"(c.wa[i])); }
    }
#pragma unroll
    for (int i = 0; i < 8 * NT; ++i) {
      const int n = i >> 2, st = (n & 1) ? 3 - (i & 3) : (i & 3);
      acc[st][n / NT][n % NT] = mfma16(f.w[n / NT][n % NT], f.x[st], acc[st][n / NT][n % NT]);
      if ((i & 1) == 0) issue(t, i >> 1);
      {
        const int lane = threadIdx.x & 63;
        if (t == 1 && i == 1) {
          asm volatile("" : "+v"(c.epoch), "+v"(c.xrow[0]), "+v"(c.xrow[1]));
          c.tag = __builtin_amdgcn_readfirstlane(c.epoch) + 1u;
          row_sums(c.xrow, c.rsum, c.rsq);
        }
        if (t == 1 && i == 15) {
          const u32x2 g = {row_stat(c.rsum, c.rsq), c.tag};
          __builtin_amdgcn_raw_buffer_store_b64(g, c.rsw, lane == 63 ? W_ROWST + (8 * blockIdx.x + (threadIdx.x >> 6)) * 8 : 0x80000000u, 0, 16);
        }
        if (t == 7 && i == 1) late_setup<NT>(c);
        if (t == 16 && i >= 3 && i < 9) ld_stat(c, i - 3);
        if (t == 18 && i == 0) asm volatile("" : "+v"(c.gr[0]), "+v"(c.gr[1]), "+v"(c.gr[2]), "+v"(c.gr[3]), "+v"(c.gc[0]), "+v"(c.gc[1]));
        if (t >= 18 && (i & 1) && i < 8) {
          const int ks = 8 * (t - 18) + c.w;
          const bool split = ks % 3 == 2;
          w2_stage(c.early ? EAR + c.w * 10240 + (t - 18) * 4096 : SPARE, c.wd + ks * 32 * (D * 2), split ? 4 : 16, split ? 8 : 4, i >> 1, lane);
        }
      }
      if (rd) {
        const int e = NT == 3 ? ((i & 1) ? i >> 1 : -1) : (i < 12 && i % 3 != 2 ? i - i / 3 : -1);
        if (e == 0) readW(fn, t + 1, 0);
        else if (e >= 1 && e <= 4) readX(fn, t + 1, e - 1);
        else if (e >= 5 && e < 4 + 2 * NT) readW(fn, t + 1, e - 4);
      }
      SB();
    }
  };
  Frags f0, f1;
  wait_bar<7>();
  STAMP(1);
#pragma unroll
  for (int i = 0; i < 4; ++i) readX(f0, 0, i);
#pragma unroll
  for (int i = 0; i < 2 * NT; ++i) readW(f0, 0, i);
  constexpr auto xtra = [](int t) { return t == 1 ? 1 : t == 16 ? 6 : 0; };
#define STEP(T, N, FC, FN) wait_bar<N + xtra(T - 1) + xtra(T - 2)>(); step(T, FC, FN);
  STEP(0, 4, f0, f1) STEP(1, 4, f1, f0) STEP(2, 9, f0, f1) STAMP(7); STEP(3, 8, f1, f0) STEP(4, 8, f0, f1)
  STEP(5, 8, f1, f0) STEP(6, 8, f0, f1) STEP(7, 8, f1, f0) STEP(8, 8, f0, f1) STEP(9, 8, f1, f0)
  MSTAMP(8); STEP(10, 8, f0, f1) STEP(11, 8, f1, f0) STEP(12, 8, f0, f1) MSTAMP(10); STEP(13, 8, f1, f0) STEP(14, 8, f0, f1)
  STEP(15, 8, f1, f0) MSTAMP(11); STEP(16, 7, f0, f1) MSTAMP(12);
  STEP(17, 3, f1, f0)
  MSTAMP(13);
  wait_bar<6>();
  MSTAMP(15);
  step(18, f0, f1);
#undef STEP
  wait_vl<63>(); step(19, f1, f0);
}

DI unsigned epilogue1(P1& c, f32x4 (&acc)[4][2][3], int cg, char* ws STAMP_ARGS) {
  int tid = threadIdx.x;
  asm volatile("" : "+v"(tid));
  const int lane = tid & 63, w = __builtin_amdgcn_readfirstlane(tid >> 6), sr = lane & 15, G = lane >> 4;
  const int wn = ((w >> 2) << 1) | (w & 1), wm = (w >> 1) & 1, rb = c.rb, lc01 = c.lc01, lc2 = c.lc2;
  const bool glu = cg < 9, isq = cg >= 13, nt3 = glu || w < 4;
  const unsigned tag = c.tag;
  auto stats_ok = [&]() { return __all(c.gr[0].y == tag && c.gr[1].y == tag && c.gr[2].y == tag && c.gr[3].y == tag && c.gc[0].y == tag && c.gc[1].y == tag); };
  if (!stats_ok()) {
#pragma unroll 1
    for (int spin = 0; spin < SPIN_MAX; ++spin) {
#pragma unroll
      for (int e = 0; e < 6; ++e) ld_stat(c, e);
      asm volatile("" : "+v"(c.gr[0]), "+v"(c.gr[1]), "+v"(c.gr[2]), "+v"(c.gr[3]), "+v"(c.gc[0]), "+v"(c.gc[1]));
      if (stats_ok()) break;
      __builtin_amdgcn_s_sleep(2);
    }
  }
  unsigned cs0 = c.gc[0].x, cs1 = c.gc[1].x;
  asm volatile("" : "+v"(cs0), "+v"(cs1));
  float sdv[4], rs[4], rsl[4], ce[4];
#pragma unroll
  for (int st = 0; st < 4; ++st) {
    const unsigned g = c.gr[st].x;
    sdv[st] = hi16(g); rs[st] = __builtin_amdgcn_rcpf(sdv[st]);
    rsl[st] = 1.44269504f * rs[st]; ce[st] = 1e-6f * sdv[st];
    const unsigned mm = (g & 0xffffu) * 0x10001u;
#pragma unroll
    for (int z = 0; z < 6; ++z)
      acc[st][z / 3][z % 3] = mfma16(u32x4{z < 4 ? cs0 : cs1, 0u, 0u, 0u}, u32x4{G == (z & 3) ? mm : 0u, 0u, 0u, 0u}, acc[st][z / 3][z % 3]);
  }
  STAMP(9);
  const rsrc_t rsw = c.rsw;
  const unsigned actb = W_ACT + (8 * rb + 4 * wm) * NSL * 1024 + lane * 16;
  const unsigned acts = W_ACT + (8 * rb + 4 * wm + (G & 1)) * NSL * 1024 + ((2 * (wn & 1) + (G >> 1)) * 16 + sr) * 16;
  auto store_split = [&](int pair, int ks, u32x2 a, u32x2 b) {
    const auto r0 = __builtin_amdgcn_permlane16_swap(a.x, b.x, false, false), r1 = __builtin_amdgcn_permlane16_swap(a.y, b.y, false, false);
    st128(rsw, acts + (2 * pair * NSL + ks) * 1024, u32x4{r0[0], r1[0], r0[1], r1[1]});
  };
  auto phi4 = [&](int st, f32x4 v) {
    return u32x2{pk2(phi(v[0], rs[st], rsl[st], ce[st]), phi(v[1], rs[st], rsl[st], ce[st])), pk2(phi(v[2], rs[st], rsl[st], ce[st]), phi(v[3], rs[st], rsl[st], ce[st]))};
  };
  auto glu4 = [&](int st, int i) {
    float h[4];
#pragma unroll
    for (int j = 0; j < 4; ++j) {
      const float e = __builtin_amdgcn_exp2f(acc[st][0][i][j] * -rsl[st]);
      h[j] = acc[st][1][i][j] * __builtin_amdgcn_rcpf(__builtin_fmaf(e, sdv[st], sdv[st]));
    }
    return u32x2{pk2(h[0], h[1]), pk2(h[2], h[3])};
  };
  if (glu) {
#pragma unroll
    for (int pair = 0; pair < 2; ++pair) {
#pragma unroll
      for (int st = 2 * pair; st < 2 * pair + 2; ++st) {
        const u32x2 a = glu4(st, 0), b = glu4(st, 1);
        st128(rsw, actb + (st * NSL + 6 * cg + (lc01 >> 5)) * 1024, u32x4{a.x, a.y, b.x, b.y});
      }
      store_split(pair, 6 * cg + (lc2 >> 5), glu4(2 * pair, 2), glu4(2 * pair + 1, 2));
    }
  } else if (isq) {
    const int sp0 = 54 + 5 * (cg - 13);
#pragma unroll
    for (int sd = 0; sd < 2; ++sd)
#pragma unroll
      for (int pair = 0; pair < 2; ++pair) {
#pragma unroll
        for (int st = 2 * pair; st < 2 * pair + 2; ++st) {
          const u32x2 a = phi4(st, acc[st][sd][0]), b = phi4(st, acc[st][sd][1]);
          st128(rsw, actb + (st * NSL + sp0 + 10 * sd + (lc01 >> 5)) * 1024, u32x4{a.x, a.y, b.x, b.y});
        }
        if (nt3) store_split(pair, sp0 + 10 * sd + 2, phi4(2 * pair, acc[2 * pair][sd][2]), phi4(2 * pair + 1, acc[2 * pair + 1][sd][2]));
      }
  } else {
#pragma unroll
    for (int st = 0; st < 4; ++st) {
      const unsigned rowa = KVI + (64 * wm + 16 * st + sr) * 640;
#pragma unroll
      for (int i = 0; i < 3; ++i) {
        if (i == 2 && !nt3) continue;
        const int u = ((i < 2 ? lc01 + 16 * i : lc2) >> 2) + G;
        const f32x4 v = acc[st][1][i];
        *(LAS u32x2*)(rowa + ((u ^ sr) << 3)) = phi4(st, acc[st][0][i]);
        *(LAS u32x2*)(rowa + (((u + 40) ^ sr) << 3)) = u32x2{pk2(v[0] * rs[st], v[1] * rs[st]), pk2(v[2] * rs[st], v[3] * rs[st])};
      }
    }
    wait_bar<63>();
    const int hf = lane >> 5, q = (lane >> 2) & 3, p = lane & 3;
    const int uk = min(5 * w + 4 * (G & 1) + p, 39);
    unsigned rk[2], rv[2];
#pragma unroll
    for (int t = 0; t < 2; ++t) {
      const int row = 8 * hf + 4 * t + q;
      rk[t] = KVI + row * 640 + ((uk ^ row) << 3); rv[t] = KVI + row * 640 + (((uk + 40) ^ row) << 3);
    }
    const bool one = (lane & 31) >= 20;
    f32x16 kv, kv2;
#pragma unroll
    for (int i = 0; i < 16; ++i) { kv[i] = 0.f; kv2[i] = 0.f; }
#pragma unroll
    for (int ks = 0; ks < 8; ++ks) {
      u32x4 vf = trfrag(rv[0] + ks * 10240, rv[1] + ks * 10240);
#pragma unroll
      for (int i = 0; i < 4; ++i) vf[i] = one ? 0x3f803f80u : vf[i];
      if (ks & 1) kv2 = mfma32(trfrag(rk[0] + ks * 10240, rk[1] + ks * 10240), vf, kv2);
      else kv = mfma32(trfrag(rk[0] + ks * 10240, rk[1] + ks * 10240), vf, kv);
    }
    kv += kv2;
#pragma unroll
    for (int j = 0; j < 3; ++j)
      if ((lane & 31) <= 20 && (j < 2 || hf == 0)) st128(rsw, W_KVP + ((rb * 32 + 8 * (cg - 9) + w) * 3 + j) * 1024 + lane * 16, __builtin_bit_cast(u32x4, f32x4{kv[4 * j], kv[4 * j + 1], kv[4 * j + 2], kv[4 * j + 3]}));
  }
  STAMP(3);
  drain_bar();
  if (w == 0) {
    if (lane == 0) st_flag(ws + W_FLAG1 + rb * 128 + cg * 4, tag);
    if (!glu && !isq && lane >= 8 && lane < 16) st_flag(ws + W_FLAGKV + ((cg - 9) * 8 + lane - 8) * 128 + rb * 4, tag);
  }
  return tag;
}

DI unsigned phase1(int rb, int cg, int cgp, const u16* x, const u16* wqkv, const u16* wg, const u16* wu, const u16* wd, char* ws STAMP_ARGS) {
  const int tid = threadIdx.x, lane = tid & 63, w = __builtin_amdgcn_readfirstlane(tid >> 6);
  const int wn = ((w >> 2) << 1) | (w & 1), wm = (w >> 1) & 1;
  P1 c;
  const int side = w & 1, oct = w >> 1, rho = lane >> 3, pi = lane & 7;
  c.voffx = (8 * w + rho) * (D * 2) + ((pi ^ (4 * (w & 1) + (rho >> 1))) << 4);
  c.xbase = (const char*)x + (size_t)rb * 128 * D * 2;
  c.wdst = WS + (side * 12 + oct) * 1024; c.xdst = XS + w * 1024;
  issueX(c, 0, 0); issueX(c, 0, 1);
  const bool glu = cg < 9, isq = cg >= 13;
  auto col0 = [&](int sd) { return glu ? 192 * cg : isq ? 160 * (cg - 13) + 320 * sd : 640 + 640 * sd + 160 * (cg - 9); };
  const int shift = (!glu && !(cg & 1)) ? 32 : 0;
  c.wbase = glu ? (side ? (const char*)wu : (const char*)wg) : (const char*)wqkv;
  const int pitch = glu ? F * 2 : 3 * D * 2;
  c.voff = (col0(side) - shift + 16 * ((pi >> 1) ^ (2 * (oct & 1) | ((rho >> 1) & 1)))) * 2 + (8 * oct + rho) * pitch + (pi & 1) * 16;
  c.wstep = 32 * pitch;
  issueW1(c, 0, 0); issueW1(c, 0, 1); issueW1(c, 0, 2);
  STAMP(5);
  SB();
  const int sr = lane & 15, G = lane >> 4;
  const int lc01 = 32 * (3 * (wn >> 1) + (wn & 1)), lc2 = 32 * (3 * (wn >> 1) + 2) + 16 * (wn & 1);
  c.xa[0] = XS + (64 * wm + sr) * 128 + ((G ^ (sr >> 1)) << 4); c.xa[1] = c.xa[0] ^ 64;
  {
    const int q = (lane >> 2) & 3, p = lane & 3;
    const unsigned b0 = WS + G * 1024 + q * 128 + p * 8, sw = 2 * (G & 1) | (q >> 1);
#pragma unroll
    for (int i = 0; i < 3; ++i) { const int lc = (i < 2 ? lc01 + 16 * i : lc2) + shift; c.wa[i] = b0 + (lc >> 6) * 4096 + ((((lc >> 4) & 3) ^ sw) << 5); }
  }
  asm volatile("" : "+v"(c.xa[0]), "+v"(c.xa[1]), "+v"(c.wa[0]), "+v"(c.wa[1]), "+v"(c.wa[2]));
  SB();
  issueW1(c, 1, 0); issueW1(c, 1, 1); issueW1(c, 1, 2); issueX(c, 1, 0);
  {
    const char* xr = (const char*)x + (size_t)(8 * blockIdx.x + w) * (D * 2);
    gload<0>(c.xrow[0], min(lane, 39) * 32, xr); gload<16>(c.xrow[1], min(lane, 39) * 32, xr);
    gload32(c.epoch, W_EPOCH, ws);
  }
  SB();
  const bool nt3 = glu || w < 4;
  c.rsw = mk_rsrc(ws);
  c.ws = ws; c.early = glu || isq; c.rb = rb; c.lc01 = lc01; c.lc2 = lc2; c.w = w;
  c.cb0 = col0(0) + (glu ? 0 : 2 * F); c.cbd = glu ? F : isq ? 320 : 640;
  c.wd = (const char*)wd + cgp * 128;
  f32x4 acc[4][2][3];
#pragma unroll
  for (int i = 0; i < 24; ++i) acc[i / 6][(i / 3) & 1][i % 3] = f32x4{0.f, 0.f, 0.f, 0.f};
  if (nt3) mainloop<3>(c, acc STAMP_PASS); else mainloop<2>(c, acc STAMP_PASS);
  STAMP(2);
  return epilogue1(c, acc, cg, ws STAMP_PASS);
}

DI void o_share(int P, int rbo, int nrb, unsigned tag, char* ws STAMP_ARGS) {
  const int tid = threadIdx.x, lane = tid & 63, w = __builtin_amdgcn_readfirstlane(tid >> 6);
  const int sr = lane & 15, G = lane >> 4;
  const rsrc_t rsw = mk_rsrc(ws);
  const unsigned l16 = lane * 16;
  {
    if (w == 0) poll(lane < 16 ? ws + W_FLAGKV + ((P >> 2) * 8 + (rbo & 7)) * 128 + lane * 4 : ws + W_FLAG1 + (rbo + lane - 16) * 128 + (13 + ((P >> 2) & 1)) * 4,
                     lane < 16 + nrb, tag);
    wait_bar<63>();
    XSTAMP(10);
    const int s0 = (40 * P) / 32;
    const unsigned tile = W_ACT + (8 * rbo + w) * NSL * 1024;
    u32x4 pqf[3][2], part[16];
    const int hh = w >= 3, pj = hh ? w - 3 : w;
    const bool pact = w < 6 && (lane & 31) <= 20 && (pj < 2 || lane < 32);
    if (pact) {
#pragma unroll
      for (int i = 0; i < 16; ++i) part[i] = ld128s(rsw, l16, W_KVP + ((i * 32 + 2 * P + hh) * 3 + pj) * 1024);
    }
#pragma unroll
    for (int r = 0; r < 3; ++r)
#pragma unroll
      for (int i = 0; i < 2; ++i) pqf[r][i] = ld128s(rsw, l16, tile + (min(r, nrb - 1) * 8 * NSL + 54 + s0 + i) * 1024);
    SB();
    if (pact) {
      f32x4 t = __builtin_bit_cast(f32x4, part[0]);
#pragma unroll
      for (int i = 1; i < 16; ++i) t += __builtin_bit_cast(f32x4, part[i]);
      *(LAS u32x2*)(T2 + hh * 1024 + (lane & 31) * 48 + (8 * pj + 4 * (lane >> 5)) * 2) = u32x2{pk2(t[0], t[1]), pk2(t[2], t[3])};
    }
    if (tid == 448) *(LAS u32x2*)(T2 + 2048) = u32x2{0u, 0u};
    OSTAMP(2);
    wait_bar<63>();
    OSTAMP(3);
    u32x4 a[8];
#pragma unroll
    for (int i = 0; i < 8; ++i) {
      const int b = i >> 2, sp = s0 + ((i >> 1) & 1);
      const int e = (i & 1) == 0 ? sr : (sr & 3) == b ? 20 : ((sr & 2) && (sr >> 3) == b) ? 16 + 2 * ((sr >> 2) & 1) + (sr & 1) : -1;
      u32x2 v[2];
#pragma unroll
      for (int hl = 0; hl < 2; ++hl) {
        const int d = 32 * sp + (sp % 5 == 2 ? 8 * G + 4 * hl : 16 * hl + 4 * G) - 20 * (2 * P + b);
        v[hl] = *(LAS u32x2*)((e >= 0 && d >= 0 && d < 20) ? T2 + b * 1024 + e * 48 + d * 2 : T2 + 2048);
      }
      a[i] = u32x4{v[0].x, v[0].y, v[1].x, v[1].y};
    }
    OSTAMP(5);
#pragma unroll
    for (int r = 0; r < 3; ++r) {
      if (r == 2 && nrb < 3) break;
      const f32x4 z = {0.f, 0.f, 0.f, 0.f};
      const f32x4 na = mfma16(a[2], pqf[r][1], mfma16(a[0], pqf[r][0], z)), nb = mfma16(a[6], pqf[r][1], mfma16(a[4], pqf[r][0], z));
      const f32x4 xx = mfma16(a[7], pqf[r][1], mfma16(a[5], pqf[r][0], mfma16(a[3], pqf[r][1], mfma16(a[1], pqf[r][0], z))));
      const float ra = __builtin_amdgcn_rcpf(xx[0] + 1e-6f), rb = __builtin_amdgcn_rcpf(xx[1] + 1e-6f), rl = G < 2 ? ra : rb;
      const unsigned tr = tile + r * 8 * NSL * 1024;
      st128(rsw, tr + (74 + P) * 1024 + l16, u32x4{pk2(na[0] * ra, na[1] * ra), pk2(na[2] * ra, na[3] * ra), pk2(nb[0] * rb, nb[1] * rb), pk2(nb[2] * rb, nb[3] * rb)});
      st32(rsw, tr + (90 + (P >> 2)) * 1024 + ((P & 3) * 16 + sr) * 16 + 4 * G, pk2(xx[2] * rl, xx[3] * rl));
    }
    drain_bar();
    if (w == 0 && lane < nrb) st_flag(ws + W_FLAGO + (rbo + lane) * 128 + P * 4, tag);
  }
}

struct W2 {
  const char *wd, *src6, *src7, *src8, *src9;
  int w, so6, so7, hs6, gs6, gs7, gs8, gs9;
  unsigned ra[2][4];
  DI void init(const u16* wd_, const u16* wo_, int cgp) {
    w = __builtin_amdgcn_readfirstlane(threadIdx.x >> 6);
    wd = (const char*)wd_ + cgp * 128;
    const char* wo = (const char*)wo_ + cgp * 128;
    so7 = slot_o(w, 7);
    auto osrc = [&](int oi) { oi = min(oi, 19); return wo + (oi >= 16 ? 160 * (oi - 16) + 16 : 40 * oi) * (D * 2); };
    auto ogs = [&](int oi) { return oi >= 16 ? 40 : 4; };
    const bool h6 = w < 6, split = (48 + w) % 3 == 2;
    so6 = h6 ? 48 + w : 74 + so7 - 1;
    src6 = h6 ? wd + (48 + w) * 32 * (D * 2) : osrc(so7 - 1);
    hs6 = h6 ? (split ? 4 : 16) : 20; gs6 = h6 ? (split ? 8 : 4) : ogs(so7 - 1);
    src7 = osrc(so7); src8 = osrc(so7 + 1); src9 = osrc(so7 + 2);
    gs7 = ogs(so7); gs8 = ogs(so7 + 1); gs9 = ogs(so7 + 2);
    asm volatile("" : "+s"(src6), "+s"(src7), "+s"(src8), "+s"(src9), "+s"(so6), "+s"(so7), "+s"(hs6), "+s"(gs6), "+s"(gs7), "+s"(gs8), "+s"(gs9));
  }
  DI unsigned lds(int s) const { return ((s >> 1) & 1) ? LAT + w * 8192 + (s & 1) * 4096 : EAR + w * 10240 + (s & 1) * 4096; }
  DI void stage1(int s, int o, int lane) const {
    const bool split = (8 * s + w) % 3 == 2;
    if (s < 6) w2_stage(lds(s), wd + (8 * s + w) * 32 * (D * 2), split ? 4 : 16, split ? 8 : 4, o, lane);
    else w2_stage(lds(s), s == 6 ? src6 : s == 7 ? src7 : s == 8 ? src8 : src9, s == 6 ? hs6 : 20, s == 6 ? gs6 : s == 7 ? gs7 : s == 8 ? gs8 : gs9, o, lane);
  }
  DI void stage(int s) const {
    int lane = __builtin_amdgcn_mbcnt_hi(~0u, __builtin_amdgcn_mbcnt_lo(~0u, 0u));
    asm volatile("" : "+v"(lane));
    stage1(s, 0, lane); stage1(s, 1, lane); stage1(s, 2, lane); stage1(s, 3, lane);
  }
  DI u32x4 frag(int s, int n) const { const unsigned a = ra[(s >> 1) & 1][n] + (s & 1) * 4096; return trfrag(a, a + 512); }
  DI void frags(u32x4 (&d)[4], int s) const {
#pragma unroll
    for (int n = 0; n < 4; ++n) d[n] = frag(s, n);
  }
  DI int aslot(int s) const { return s < 6 ? 8 * s + w : s == 6 ? so6 : 74 + min(so7 + s - 7, 19); }
};

template <int NS> DI void gemm2(W2& W, int st0, int cgp, unsigned tag, const u16* x, u16* out, char* ws STAMP_ARGS) {
  int tid = threadIdx.x;
  asm volatile("" : "+v"(tid));
  const int lane = tid & 63, w = W.w, sr = lane & 15, G = lane >> 4;
#pragma unroll
  for (int i = 0; i < 8; ++i) {
    W.ra[i >> 2][i & 3] = W.lds(i >> 2 << 1) + ((G * 1024 + ((lane >> 2) & 3) * 128 + ((2 * (G & 1) | ((lane >> 3) & 1)) << 5) + (lane & 3) * 8) ^ ((i & 3) << 5));
    asm volatile("" : "+v"(W.ra[i >> 2][i & 3]));
  }
  u32x4 wr[4][4];
  wait_v<8>();
  W.frags(wr[0], 0); W.frags(wr[1], 1);
  wait_vl<63>();
  W.stage(4); W.stage(5);
  wait_v<8>();
  W.frags(wr[2], 2);
  if (NS == 5) W.frags(wr[3], 3);
  wait_vl<63>();
  W.stage(6);
  if (NS == 5) W.stage(7);
  XSTAMP(8);
  const rsrc_t rsw = mk_rsrc(ws);
  const unsigned l16 = lane * 16;
  const unsigned fo = ((lane & 16) ? (st0 + NS - 1) >> 3 : st0 >> 3) * 128 + sr * 4;
  if (w == 0) poll(ws + W_FLAG1 + fo, sr < 9, tag);
  wait_bar<63>();
  XSTAMP(11);
  const unsigned abase = W_ACT + st0 * NSL * 1024;
  constexpr int AH = NS == 5 ? 3 : 2;
  u32x4 a[10][NS], wl[4];
  auto issueA1 = [&](int s, int st) { a[s][st] = ld128s(rsw, l16, abase + (W.aslot(s) + st * NSL) * 1024); };
#pragma unroll
  for (int e = 0; e < AH * NS; ++e) issueA1(e / NS, e % NS);
  SB();
  f32x4 acc[NS][4];
#pragma unroll
  for (int i = 0; i < 4 * NS; ++i) acc[i >> 2][i & 3] = f32x4{0.f, 0.f, 0.f, 0.f};
  unsigned oflag;
  auto vm = [&](int s, int e) {
    if (s + AH <= 5 && e < NS) issueA1(s + AH, e);
    if (s == 2 && e == NS) oflag = ld32(rsw, W_FLAGO + fo);
    if (NS != 5 && s == 4 && e == 0) W.stage(7);
    if (s == 4 && e < (NS == 5 ? 2 : 1) * NS) issueA1(6 + e / NS, e % NS);
    if (s == 5) { if (e == 0) W.stage(8); else if (e < 4) {} else if (e < 4 + NS) issueA1(NS == 5 ? 8 : 7, e - 4); }
    if (NS != 5 && s == 6 && e < NS) issueA1(8, e);
  };
  auto slot = [&](int s, const u32x4 (&wc)[4]) {
#pragma unroll
    for (int i = 0; i < 4 * NS; ++i) {
      const int n = i / NS, st = (n & 1) ? NS - 1 - i % NS : i % NS;
      acc[st][n] = mfma16(wc[n], a[s][st], acc[st][n]);
      if ((i & 1) == 0) vm(s, i >> 1);
      if (s >= (NS == 5 ? 3 : 2) && s <= 7 && i % NS == NS - 1) wl[n] = W.frag(s + 1, n);
      SB();
    }
  };
  P2STAMP(0); slot(0, wr[0]);
  P2STAMP(1); slot(1, wr[1]);
  P2STAMP(2); slot(2, wr[2]);
  P2STAMP(3); slot(3, NS == 5 ? wr[3] : wl);
  P2STAMP(4);
  if (!__all(oflag == tag)) poll(ws + W_FLAGO + fo, true, tag);
  SB();
  XSTAMP(15);
  slot(4, wl);
  slot(5, wl);
  const bool ten = w == 4 || w == 5;
  auto ask9 = [&]() {
    if (ten) {
      W.stage(9);
#pragma unroll
      for (int e = 0; e < NS; ++e) issueA1(9, e);
    }
  };
  if (NS == 5) ask9();
  slot(6, wl);
  XSTAMP(12);
  int tz = __builtin_amdgcn_mbcnt_hi(~0u, __builtin_amdgcn_mbcnt_lo(~0u, 0u));
  asm volatile("" : "+v"(tz));
  const unsigned eo = (16 * st0 + (tz & 15)) * D + 64 * cgp + 4 * ((tz >> 4) & 3);
  u32x2 xres[3];
#pragma unroll
  for (int i = 0; i < 3; ++i) {
    const int tl = min(w + 8 * i, 4 * NS - 1);
    xres[i] = *(const GAS u32x2*)(x + 16 * (tl >> 2) * D + 16 * (tl & 3) + eo);
  }
  wait_v<(NS == 5 ? NS : 2 * NS) + 3>();
  if (NS != 5) ask9();
  slot(7, wl);
  slot(8, wl);
  if (ten) {
    wait_v<0>();
    W.frags(wl, 9);
    slot(9, wl);
  }
  XSTAMP(13);
  auto finish = [&](int i, int tl, unsigned base) {
    f32x4 v[8];
#pragma unroll
    for (int j = 0; j < 8; ++j) v[j] = *(LAS f32x4*)(base + j * 1024 + l16);
    const f32x4 s = ((v[0] + v[1]) + (v[2] + v[3])) + ((v[4] + v[5]) + (v[6] + v[7]));
    const u32x2 o = {pk2(s[0] + lo16(xres[i].x), s[1] + hi16(xres[i].x)), pk2(s[2] + lo16(xres[i].y), s[3] + hi16(xres[i].y))};
    *(GAS u32x2*)(out + 16 * (tl >> 2) * D + 16 * (tl & 3) + eo) = o;
  };
  wait_bar<63>();
  OSTAMP(7);
#pragma unroll
  for (int tl = 0; tl < (NS == 5 ? 20 : 16); ++tl) *(LAS f32x4*)((tl * 8 + w) * 1024 + l16) = acc[tl >> 2][tl & 3];
  OSTAMP(9);
  wait_bar<63>();
  OSTAMP(6);
  finish(0, w, w * 8192);
  finish(1, w + 8, (w + 8) * 8192);
  if (NS == 5) {
    if (w < 4) finish(2, w + 16, (w + 16) * 8192);
  } else {
    wait_bar<63>();
#pragma unroll
    for (int tl = 16; tl < 4 * NS; ++tl) *(LAS f32x4*)(((tl - 16) * 8 + w) * 1024 + l16) = acc[tl >> 2][tl & 3];
    wait_bar<63>();
    finish(2, w + 16, w * 8192);
  }
}

__global__ __launch_bounds__(512, 2) void fused(const u16* __restrict__ x, const u16* __restrict__ wqkv, const u16* __restrict__ wg,
                                                const u16* __restrict__ wu, const u16* __restrict__ wd, const u16* __restrict__ wo,
                                                u16* __restrict__ out, char* __restrict__ ws) {
  asm volatile("" : "+s"(x), "+s"(wqkv), "+s"(wg), "+s"(wu), "+s"(wd), "+s"(wo), "+s"(out), "+s"(ws));
  const int xcd = blockIdx.x & 7, idx = blockIdx.x >> 3;
  STAMP_DECL
  STAMP(0);
  unsigned tag;
  const int half = xcd & 1;
  const int tile = xcd < 5 ? 32 * xcd + idx : idx < 30 ? 160 + 30 * (xcd - 5) + idx : -1;
  const int rg = max(tile, 0) / 10, cgp = max(tile, 0) - 10 * rg;
  int P = -1, jj = 0;
  bool early = false;
  if (idx >= 30) {
    tag = helper(half, 2 * (xcd >> 1) + idx - 30, x, wqkv, wg, wu, ws STAMP_PASS);
    P = 2 * xcd + idx - 30;
    wait_bar<63>();
  } else {
    const int i = idx / 7, j = idx - 7 * i;
    const int cg = idx >= 28 ? 4 : j < 4 ? 5 * half + j : j < 6 ? 5 + 2 * half + j : 13 + half;
    const int rb = 4 * (xcd >> 1) + (idx >= 28 ? 2 * half + idx - 28 : i);
    if (cg >= 9) { const int r = 3 * i + j - 4; P = 2 * xcd + (r & 1); jj = 1 + (r >> 1); }
    early = cg < 9 || cg >= 13;
    tag = phase1(rb, cg, cgp, x, wqkv, wg, wu, wd, ws STAMP_PASS);
  }
  STAMP(4);
  W2 W;
  W.init(wd, wo, cgp);
  if (!early) { W.stage(0); W.stage(1); }
  W.stage(2); W.stage(3);
  if (P >= 0) o_share(P, jj < 2 ? 3 * jj : 2 + 2 * jj, jj < 2 ? 3 : 2, tag, ws STAMP_PASS);
  if (tile >= 0) {
    if (rg < 3) gemm2<6>(W, 6 * rg, cgp, tag, x, out, ws STAMP_PASS);
    else gemm2<5>(W, 3 + 5 * rg, cgp, tag, x, out, ws STAMP_PASS);
  } else if (blockIdx.x == 255 && threadIdx.x < 64) {
    const int lane = threadIdx.x & 63;
#pragma unroll
    for (int i = 0; i < 4; ++i) {
      poll(ws + W_FLAG1 + (4 * i + (lane >> 4)) * 128 + (lane & 15) * 4, (lane & 15) < 15, tag);
      poll(ws + W_FLAGO + (4 * i + (lane >> 4)) * 128 + (lane & 15) * 4, true, tag);
    }
    if (lane == 0) st_flag(ws + W_EPOCH, tag);
  }
  STAMP(14);
  STAMP_OUT
}

extern "C" void kernel_launch(void* const* d_in, const int* in_sizes, int n_in, void* d_out, int out_size, void* d_ws, size_t ws_size,
                              hipStream_t stream) {
  const u16* x = (const u16*)d_in[0]; const u16* wqkv = (const u16*)d_in[1]; const u16* wo = (const u16*)d_in[2];
  const u16* wg = (const u16*)d_in[4]; const u16* wu = (const u16*)d_in[5]; const u16* wd = (const u16*)d_in[6];
  (void)hipFuncSetAttribute((const void*)fused, hipFuncAttributeMaxDynamicSharedMemorySize, LDS_BYTES);
  fused<<<256, 512, LDS_BYTES, stream>>>(x, wqkv, wg, wu, wd, wo, (u16*)d_out, (char*)d_ws);
}
```

```cpp
#define STAMPS
#define NSTAMP 16
#define PBLOCK 8
#define PROBE 0
#define PCYC 0
#include <hip/hip_runtime.h>
#include <hip/hip_bf16.h>
#define PF_EARLY 16
#define PF_LOOP 10
#define SPREAD 2
#define PRIO 1
#define WARM 1
#define JOBABL 0
#define ACTMODE 0
#define POLL_SLEEP 4
typedef unsigned short u16;
typedef unsigned long long u64;
typedef short s16x4 __attribute__((ext_vector_type(4)));
typedef __bf16 bf16x2 __attribute__((ext_vector_type(2)));
typedef __bf16 bf16x8 __attribute__((ext_vector_type(8)));
typedef float f32x16 __attribute__((ext_vector_type(16)));
typedef float f32x4 __attribute__((ext_vector_type(4)));
typedef unsigned u32x4 __attribute__((ext_vector_type(4)));
typedef unsigned u32x2 __attribute__((ext_vector_type(2)));
#define LAS __attribute__((address_space(3)))
#define GAS __attribute__((address_space(1)))
#define DI __device__ __forceinline__
#define RLX_AGENT __ATOMIC_RELAXED, __HIP_MEMORY_SCOPE_AGENT

constexpr int S = 2048, D = 640, F = 1728;
constexpr int NSL = 94;
constexpr int XS = 0, WS = 49152, LDS_BYTES = 163840;
constexpr int KVI = 16384;
constexpr int SPIN_MAX = 1 << 14;
constexpr unsigned W_EPOCH = 0, W_FLAG1 = 1024  , W_FLAGO = 81920  ,
                   W_FLAGKV = 4096  ,
                   W_ROWST = 16384, W_COLST = W_ROWST + 2048 * 8, W_ACT = 1 << 20  ,
                   W_KVP = 14 << 20  , W_STAMP = 20 << 20;
constexpr int SPARE = 147456;
constexpr int EAR = 16384, LAT = 98304;
constexpr int T2 = 0;

constexpr u64 TB0 = 0x322c2620120c0600ull, TB1 = 0x806c6660524c4640ull, TB2 = 0x5c583c381c188c86ull, TB3 = 0x96929d9a7d7a7672ull;

DI float rbf(float f) { return (float)(__bf16)f; }
DI unsigned pk2(float a, float b) { bf16x2 v = {(__bf16)a, (__bf16)b}; return __builtin_bit_cast(unsigned, v); }
DI float lo16(unsigned u) { return __uint_as_float(u << 16); }
DI float hi16(unsigned u) { return __uint_as_float(u & 0xffff0000u); }
DI float dot2(unsigned a, unsigned b, float c) { return __builtin_amdgcn_fdot2_f32_bf16(__builtin_bit_cast(bf16x2, a), __builtin_bit_cast(bf16x2, b), c, false); }
DI f32x16 mfma32(u32x4 a, u32x4 b, f32x16 c) { return __builtin_amdgcn_mfma_f32_32x32x16_bf16(__builtin_bit_cast(bf16x8, a), __builtin_bit_cast(bf16x8, b), c, 0, 0, 0); }
DI f32x4 mfma16(u32x4 a, u32x4 b, f32x4 c) { return __builtin_amdgcn_mfma_f32_16x16x32_bf16(__builtin_bit_cast(bf16x8, a), __builtin_bit_cast(bf16x8, b), c, 0, 0, 0); }
DI void mfma16_acc(f32x4& c, u32x4 a, u32x4 b) { asm volatile("v_mfma_f32_16x16x32_bf16 %0, %1, %2, %0" : "+v"(c) : "v"(a), "v"(b)); }
DI void mfma16_new(f32x4& c, u32x4 a, u32x4 b) { asm volatile("v_mfma_f32_16x16x32_bf16 %0, %1, %2, 0" : "=&v"(c) : "v"(a), "v"(b)); }
DI void dma16(unsigned ldsaddr, unsigned voff, const char* sbase) {
  asm volatile("s_mov_b32 m0, %0\n\ts_nop 0\n\tglobal_load_lds_dwordx4 %1, %2" ::"s"(ldsaddr), "v"(voff), "s"(sbase));
}
DI void dma16p(int go, unsigned ldsaddr, unsigned voff, const char* sbase) {
  asm volatile("s_cmp_eq_u32 %3, 0\n\ts_cbranch_scc1 .Lskip%=\n\ts_mov_b32 m0, %0\n\ts_nop 0\n\tglobal_load_lds_dwordx4 %1, %2\n.Lskip%=:"
               ::"s"(ldsaddr), "v"(voff), "s"(sbase), "s"(go) : "scc");
}
DI void aload(u32x4& d, unsigned voff, const char* sbase) { asm volatile("global_load_dwordx4 %0, %1, %2 sc1" : "=v"(d) : "v"(voff), "s"(sbase)); }
DI void gload32(unsigned& d, unsigned voff, const char* sbase) { asm volatile("global_load_dword %0, %1, %2" : "=v"(d) : "v"(voff), "s"(sbase)); }
template <int OFF> DI void gload(u32x4& d, unsigned voff, const char* sbase) { asm volatile("global_load_dwordx4 %0, %1, %2 offset:%3" : "=v"(d) : "v"(voff), "s"(sbase), "i"(OFF)); }
DI void aload32(unsigned& d, unsigned voff, const char* sbase) { asm volatile("global_load_dword %0, %1, %2 sc1" : "=v"(d) : "v"(voff), "s"(sbase)); }
DI void sload_epoch(unsigned& e, const char* ws) { asm volatile("s_load_dword %0, %1, 0x0\n\ts_waitcnt lgkmcnt(0)" : "=s"(e) : "s"(ws)); }
DI u32x2 trread(unsigned addr) { return __builtin_bit_cast(u32x2, __builtin_amdgcn_ds_read_tr16_b64_v4i16((LAS s16x4*)addr)); }
DI u32x4 trfrag(unsigned a0, unsigned a1) { const u32x2 lo = trread(a0), hi = trread(a1); return u32x4{lo.x, lo.y, hi.x, hi.y}; }
DI u32x4 ldsread128(unsigned addr) { return *(LAS u32x4*)addr; }
DI void st_gran(char* p, unsigned tag, unsigned v) { __hip_atomic_store((GAS u64*)p, ((u64)tag << 32) | v, RLX_AGENT); }
DI unsigned ld_flag(const char* p) { return __hip_atomic_load((const GAS unsigned*)p, RLX_AGENT); }
DI void st_flag(char* p, unsigned v) { __hip_atomic_store((GAS unsigned*)p, v, RLX_AGENT); }
typedef __amdgpu_buffer_rsrc_t rsrc_t;
DI rsrc_t mk_rsrc(char* ws) { return __builtin_amdgcn_make_buffer_rsrc(ws, 0, 0x7fffffff, 0x00020000); }
DI void st128(rsrc_t rs, unsigned off, u32x4 v) { __builtin_amdgcn_raw_buffer_store_b128(v, rs, off, 0, 16); }
DI void st32(rsrc_t rs, unsigned off, unsigned v) { __builtin_amdgcn_raw_buffer_store_b32(v, rs, off, 0, 16); }
DI u32x4 ld128(rsrc_t rs, unsigned off) { return __builtin_amdgcn_raw_buffer_load_b128(rs, off, 0, 16); }
DI u32x4 ld128s(rsrc_t rs, unsigned voff, unsigned soff) { return __builtin_amdgcn_raw_buffer_load_b128(rs, voff, soff, 16); }
DI u32x2 ld64(rsrc_t rs, unsigned off) { return __builtin_amdgcn_raw_buffer_load_b64(rs, off, 0, 16); }
DI unsigned ld32(rsrc_t rs, unsigned off) { return __builtin_amdgcn_raw_buffer_load_b32(rs, off, 0, 16); }
DI float exp2_sat(float t) { return __builtin_amdgcn_fmed3f(__builtin_amdgcn_exp2f(t), 0.f, 1.f); }
DI float phi(float a, float rs, float rsl, float ce) { return __builtin_fmaf(fmaxf(a, ce), rs, exp2_sat(a * rsl)); }

#define SB() __builtin_amdgcn_sched_barrier(0)
#define FENCE() asm volatile("" ::: "memory")
#define WCNT(V, L) (((V) & 15) | (7 << 4) | ((L) << 8) | (((V) >> 4) << 14))
template <int N> DI void wait_v() { SB(); FENCE(); __builtin_amdgcn_s_waitcnt(WCNT(N, 15)); FENCE(); SB(); }
template <int N> DI void wait_vl() { SB(); FENCE(); __builtin_amdgcn_s_waitcnt(WCNT(N, 0)); FENCE(); SB(); }
template <int N> DI void wait_bar() { SB(); FENCE(); __builtin_amdgcn_s_waitcnt(WCNT(N, 0)); __builtin_amdgcn_s_barrier(); FENCE(); SB(); }
DI void drain_bar() { SB(); asm volatile("s_waitcnt vmcnt(0) lgkmcnt(0)\n\ts_barrier" ::: "memory"); SB(); }
DI void poll(const char* p, bool act, unsigned tag) {
#pragma unroll 1
  for (int spin = 0; spin < SPIN_MAX; ++spin) {
    if (__all(!act || ld_flag(p) == tag)) break;
    __builtin_amdgcn_s_sleep(POLL_SLEEP);
  }
  __builtin_amdgcn_fence(__ATOMIC_ACQUIRE, "wavefront");
}
#define STAMP(i) do{ SB(); u64 t_, c_; asm volatile("s_memrealtime %0\n\ts_memtime %1\n\ts_waitcnt lgkmcnt(0)":"=s"(t_),"=s"(c_)::"memory"); \
  asm volatile("v_writelane_b32 %0, %2, %4\n\tv_writelane_b32 %1, %3, %4" : "+v"(st_t), "+v"(st_c) : "s"((unsigned)t_), "s"((unsigned)c_), "i"(i)); SB();}while(0)
#define STAMP_DECL unsigned st_t = 0, st_c = 0;
#define STAMP_OUT if((threadIdx.x & 63) < 16){ unsigned* stamps = (unsigned*)(ws + W_STAMP); stamps[((blockIdx.x*8+w)*16+(threadIdx.x&63))*2]=st_t; stamps[((blockIdx.x*8+w)*16+(threadIdx.x&63))*2+1]=st_c; }
#define STAMP_ARGS , unsigned& st_t, unsigned& st_c
#define STAMP_PASS , st_t, st_c

template <int CTRL, int RM> DI float dpp_add(float v) {
  return v + __builtin_bit_cast(float, __builtin_amdgcn_update_dpp(0, __builtin_bit_cast(int, v), CTRL, RM, 0xf, true));
}
DI float wave_sum(float v) {
  v = dpp_add<0x111, 0xf>(v); v = dpp_add<0x112, 0xf>(v); v = dpp_add<0x114, 0xf>(v); v = dpp_add<0x118, 0xf>(v);
  v = dpp_add<0x142, 0xa>(v); return dpp_add<0x143, 0xc>(v);
}
DI void row_sums(const u32x4 (&xr)[2], float& s, float& q) {
  s = 0.f; q = 0.f;
#pragma unroll
  for (int i = 0; i < 8; ++i) { s = dot2(xr[i >> 2][i & 3], 0x3f803f80u, s); q = dot2(xr[i >> 2][i & 3], xr[i >> 2][i & 3], q); }
  const bool on = (threadIdx.x & 63) < 40;
  s = wave_sum(on ? s : 0.f); q = wave_sum(on ? q : 0.f);
}
DI unsigned row_stat(float s, float q) {
  const float m = s * (1.f / D);
  return pk2(-m, sqrtf(rbf(rbf(fmaxf(__builtin_fmaf(-m, m, q * (1.f / D)), 0.f)) + 1e-5f)));
}
DI unsigned helper(int half, int hr, const u16* x, const u16* wqkv, const u16* wg, const u16* wu, const u16* wd, const u16* wo, char* ws STAMP_ARGS) {
  const int tid = threadIdx.x, lane = tid & 63, w = __builtin_amdgcn_readfirstlane(tid >> 6);
  const int sub = lane >> 3, ch = lane & 7;
  unsigned tag;
  sload_epoch(tag, ws);
  {
    u32x4 xr[2];
    const char* xp = (const char*)x + (size_t)(8 * blockIdx.x + w) * (D * 2) + min(lane, 39) * 32;
    xr[0] = *(const GAS u32x4*)xp; xr[1] = *(const GAS u32x4*)(xp + 16);
    float s, q;
    row_sums(xr, s, q);
    if (lane == 63) st_gran(ws + W_ROWST + (8 * blockIdx.x + w) * 8, tag + 1u, row_stat(s, q));
    wait_v<0>();
  }
  if (w < 6) {
    const int n = hr + 8 * w, ng = 14 - half, m = n - 2 * ng;
    if (m < 15) {
      const int sl = n < ng ? 14 * half + n : m < 0 ? 27 + 14 * half + n - ng
                   : m < 5 ? 54 + (half ? (m < 2 ? 3 + m : 5 + m) : (m < 3 ? m : 2 + m)) : m < 10 ? 59 + 5 * half + m : 64 + 5 * half + m;
      const char* base = sl < 27 ? (const char*)wg + sl * 128 : sl < 54 ? (const char*)wu + (sl - 27) * 128 : (const char*)wqkv + (sl - 54) * 128;
      const int pitch = sl < 54 ? F * 2 : 3 * D * 2;
      const unsigned reg = w * 24576, vo = sub * pitch + ch * 16;
      auto chunk = [&](int i) {
#pragma unroll
        for (int j = 0; j < 8; ++j) dma16(reg + (i % 3) * 8192 + j * 1024, vo, base + (size_t)((9 - i) * 64 + j * 8) * pitch);
      };
      chunk(0); chunk(1); chunk(2);
      const unsigned ra = reg + (8 * (lane >> 5) + ((lane >> 2) & 3)) * 128 + ((lane >> 4) & 1) * 32 + (lane & 3) * 8;
      const u32x4 ones = {0x3f803f80u, 0x3f803f80u, 0x3f803f80u, 0x3f803f80u};
      f32x16 c[2];
#pragma unroll
      for (int e = 0; e < 16; ++e) { c[0][e] = 0.f; c[1][e] = 0.f; }
#pragma unroll
      for (int i = 0; i < 10; ++i) {
        if (i < 8) wait_v<16>(); else if (i == 8) wait_v<8>(); else wait_v<0>();
#pragma unroll
        for (int ks = 0; ks < 4; ++ks)
#pragma unroll
          for (int t = 0; t < 2; ++t) { const unsigned a = ra + (i % 3) * 8192 + ks * 2048 + t * 64; c[t] = mfma32(ones, trfrag(a, a + 512), c[t]); }
        wait_vl<63>();
        if (i + 3 < 10) chunk(i + 3);
      }
      const float cs = lane >> 5 ? c[1][0] : c[0][0], hi = rbf(cs);
      st_gran(ws + W_COLST + (sl * 64 + lane) * 8, tag + 1u, pk2(hi, cs - hi));
    }
  }
  STAMP(1);
  if (w >= 6) { __builtin_amdgcn_s_sleep(127); __builtin_amdgcn_s_sleep(127); }
  {
    const int b0 = ((half * 8 + hr) * 8 + w) * 24 * 1024;
#pragma unroll 1
    for (int i = 0; i < 24; ++i) {
      const int b = min(b0 + i * 1024, (F + D) * D * 2 - 1024);
      dma16(SPARE + w * 2048, lane * 16, b < F * D * 2 ? (const char*)wd + b : (const char*)wo + (b - F * D * 2));
    }
    wait_v<0>();
  }
  STAMP(2);
  return tag + 1u;
}

#define MSTAMP(i)
#define XSTAMP(i) STAMP(i)
#define KSTAMP(i)
#define OSTAMP(i)
#define P2STAMP(J)
#define XV(v) v
struct Frags { u32x4 x[4]; u32x4 w[2][3]; };
struct P1 {
  const char *xbase, *wbase, *xall, *wd;
  char* ws;
  unsigned voffx, voff, voff2, wdst, xdst, xa[2], wa[3], rowo, colo[2], tag, epoch;
  int wstep, w, cgp, rb, cb0, cbd, lc01, lc2;
  rsrc_t rsw;
  u32x2 gr[4], gc[2];
  u32x4 xrow[2];
  float rsum, rsq;
  bool early;
};
DI void w2_stage(unsigned dst, const char* src, int hs, int gs, int o, int lane, int go = -1) {
  const int rho = lane >> 3, pi = lane & 7;
  const unsigned v = (hs * (rho >> 2) + (rho & 3)) * (D * 2) + (((pi >> 1) ^ ((rho >> 1) & 1)) << 5) + (pi & 1) * 16;
  if (__builtin_constant_p(go)) dma16(dst + o * 1024, v ^ ((o & 1) << 6), src + o * gs * (D * 2));
  else dma16p(go, dst + o * 1024, v ^ ((o & 1) << 6), src + o * gs * (D * 2));
}
DI void issueX(const P1& c, int v, int half) { dma16(c.xdst + (v % 3) * 16384 + half * 8192, c.voffx, c.xbase + half * 64 * D * 2 + XV(v) * 128); }
DI void issueW1(const P1& c, int j, int pr) { dma16(c.wdst + (j & 3) * 24576 + pr * 4096, c.voff, c.wbase + (size_t)j * c.wstep + pr * 128); }
DI void ld_stat(P1& c, int e) {
  if (e < 4) c.gr[e] = ld64(c.rsw, c.rowo + e * 128);
  else if (e < 6) c.gc[e - 4] = ld64(c.rsw, c.colo[e - 4]);
}
template <int NT> DI void late_setup(P1& c) {
  const int lane = threadIdx.x & 63, sr = lane & 15, G = lane >> 4;
  c.rowo = W_ROWST + (128 * c.rb + 64 * ((threadIdx.x >> 7) & 1) + sr) * 8;
#pragma unroll
  for (int m = 0; m < 2; ++m) {
    const int z = min(4 * m + G, 5), sd = z >= 3, i = sd ? z - 3 : z;
    const int l1 = c.lc01, l2 = NT == 3 ? c.lc2 : l1, lc = i < 2 ? l1 + 16 * i : l2;
    c.colo[m] = W_COLST + (c.cb0 + (sd ? c.cbd : 0) + lc + sr) * 8;
  }
  asm volatile("" : "+v"(c.rowo), "+v"(c.colo[0]), "+v"(c.colo[1]));
}
template <int MODE> DI void mainloop(P1& c, f32x4 (&acc)[4][2][3] STAMP_ARGS) {
  constexpr int NT = MODE == 2 ? 2 : 3;
  auto readX = [&](Frags& f, int t, int st) { f.x[st] = ldsread128(c.xa[t & 1] + ((t >> 1) % 3) * 16384 + st * 2048); };
  auto readW = [&](Frags& f, int t, int n) {
    const unsigned a = c.wa[n % NT] + (t & 1) * 24576 + (n / NT) * 12288;
    f.w[n / NT][n % NT] = trfrag(a, a + 512);
  };
  auto issue = [&](int t, int e) {
    const int v = (t + 5) / 2, half = (t + 1) & 1, j = t + 4;
    if (MODE == 0) {
      if (e < 3) { if (j < 20) issueW1(c, j, e); }
      else if (e == 3) { if (v < 10) issueX(c, v, half); }
    } else if (MODE == 1) {
      if (e < 2 && v < 10) dma16(XS + (v % 3) * 16384 + half * 8192 + (2 * c.w + e) * 1024, c.voff2 ^ (e << 6), c.xbase + (half * 64 + e * 8) * D * 2 + v * 128);
    } else {
      if (e < 6 && j < 20)
        dma16(WS + (j & 3) * 24576 + e * 4096 + (c.w - 4) * 1024, c.voff2, c.wbase + (size_t)j * c.wstep + (e % 3) * 128 + (e / 3) * c.cbd * 2);
    }
  };
  auto step = [&](int t, const Frags& f, Frags& fn) {
    const bool rd = t + 1 < 20;
    if (rd && ((t + 1) & 1) == 0) {
#pragma unroll
      for (int i = 0; i < NT; ++i) { c.wa[i] = ((t + 1) & 2) ? c.wa[i] + 49152 : c.wa[i] - 49152; asm volatile("" : "+v"(c.wa[i])); }
    }
#pragma unroll
    for (int i = 0; i < 8 * NT; ++i) {
      const int n = i >> 2, st = (n & 1) ? 3 - (i & 3) : (i & 3);
      acc[st][n / NT][n % NT] = mfma16(f.w[n / NT][n % NT], f.x[st], acc[st][n / NT][n % NT]);
      if (MODE == 1) { if (i % 8 == 0) issue(t, i >> 3); } else if ((i & 1) == 0) issue(t, i >> 1);
      {
        const int lane = threadIdx.x & 63;
        if (t == 2 && i == 1) {
          asm volatile("" : "+v"(c.epoch), "+v"(c.xrow[0]), "+v"(c.xrow[1]));
          c.tag = __builtin_amdgcn_readfirstlane(c.epoch) + 1u;
          row_sums(c.xrow, c.rsum, c.rsq);
        }
        if (t == 2 && i == 15) {
          const u32x2 g = {row_stat(c.rsum, c.rsq), c.tag};
          __builtin_amdgcn_raw_buffer_store_b64(g, c.rsw, lane == 63 ? W_ROWST + (8 * blockIdx.x + (threadIdx.x >> 6)) * 8 : 0x80000000u, 0, 16);
        }
        if (t == 7 && i == 1) late_setup<NT>(c);
        if (t == 16 && i >= 3 && i < 9) ld_stat(c, i - 3);
        if (t == 18 && i == 0) asm volatile("" : "+v"(c.gr[0]), "+v"(c.gr[1]), "+v"(c.gr[2]), "+v"(c.gr[3]), "+v"(c.gc[0]), "+v"(c.gc[1]));
        if (t >= 18 && (i & 1) && i < 8) {
          const int ks = 8 * (t - 18) + c.w;
          const bool split = ks % 3 == 2;
          w2_stage(c.early ? EAR + c.w * 10240 + (t - 18) * 4096 : SPARE, c.wd + ks * 32 * (D * 2), split ? 4 : 16, split ? 8 : 4, i >> 1, lane);
        }
      }
      if (rd) {
        const int e = NT == 3 ? ((i & 1) ? i >> 1 : -1) : (i < 12 && i % 3 != 2 ? i - i / 3 : -1);
        if (e == 0) readW(fn, t + 1, 0);
        else if (e >= 1 && e <= 4) readX(fn, t + 1, e - 1);
        else if (e >= 5 && e < 4 + 2 * NT) readW(fn, t + 1, e - 4);
      }
      SB();
    }
  };
  Frags f0, f1;
  wait_bar<15>();
  STAMP(1);
#pragma unroll
  for (int i = 0; i < 4; ++i) readX(f0, 0, i);
#pragma unroll
  for (int i = 0; i < 2 * NT; ++i) readW(f0, 0, i);
  constexpr auto cnt = [](int t) {
    return (MODE == 0 ? (t <= 14 ? 4 : t == 15 ? 3 : 0) : MODE == 1 ? (t <= 14 ? 2 : 0) : (t <= 15 ? 6 : 0)) + (t == 2 ? 1 : t == 16 ? 6 : 0);
  };
#define STEP(T, FC, FN) wait_bar<cnt(T - 1) + cnt(T - 2)>(); step(T, FC, FN);
  wait_bar<12>(); step(0, f0, f1);
  wait_bar<7 + cnt(0)>(); step(1, f1, f0);
  STEP(2, f0, f1) STAMP(7); STEP(3, f1, f0) STEP(4, f0, f1)
  STEP(5, f1, f0) STEP(6, f0, f1) STEP(7, f1, f0) STEP(8, f0, f1) STEP(9, f1, f0)
  MSTAMP(8); STEP(10, f0, f1) STEP(11, f1, f0) STEP(12, f0, f1) MSTAMP(10); STEP(13, f1, f0) STEP(14, f0, f1)
  STEP(15, f1, f0) MSTAMP(11); STEP(16, f0, f1) MSTAMP(12);
  STEP(17, f1, f0)
  MSTAMP(13);
  wait_bar<6>();
  MSTAMP(15);
  step(18, f0, f1);
#undef STEP
  wait_vl<63>(); step(19, f1, f0);
}

struct W2 {
  const char *wd, *src7, *src8, *src9;
  int w, so7, gs7, gs8, gs9;
  unsigned ra[2][4];
  DI void init(const u16* wd_, int cgp) {
    w = __builtin_amdgcn_readfirstlane(threadIdx.x >> 6);
    wd = (const char*)wd_ + cgp * 128;
  }
  DI void init_o(const u16* wo_, int cgp) {
    const char* wo = (const char*)wo_ + cgp * 128;
    so7 = w < 4 ? 2 * w : 3 * w - 4;
    auto osrc = [&](int oi) { return wo + (oi >= 16 ? 160 * (oi - 16) + 16 : 40 * oi) * (D * 2); };
    auto ogs = [&](int oi) { return oi >= 16 ? 40 : 4; };
    src7 = osrc(so7); src8 = osrc(so7 + 1); src9 = osrc(so7 + 2);
    gs7 = ogs(so7); gs8 = ogs(so7 + 1); gs9 = ogs(so7 + 2);
    asm volatile("" : "+s"(src7), "+s"(src8), "+s"(src9), "+s"(so7), "+s"(gs7), "+s"(gs8), "+s"(gs9));
  }
  static DI int area(int s) { return (s >> 2) & 1 || s >= 8 ? ((s >> 1) & 1) == 0 : 0; }
  DI unsigned lds(int s) const { return area(s) ? LAT + w * 8192 + (s & 1) * 4096 : EAR + w * 10240 + (s & 1) * 4096; }
  DI int hks(int s) const { return s < 6 ? 8 * s + w : min(48 + w, 53); }
  DI void stage1(int s, int o, int lane, int go) const {
    const bool split = hks(s) % 3 == 2;
    if (s < 7) w2_stage(lds(s), wd + hks(s) * 32 * (D * 2), split ? 4 : 16, split ? 8 : 4, o, lane, go);
    else w2_stage(lds(s), s == 7 ? src7 : s == 8 ? src8 : src9, 20, s == 7 ? gs7 : s == 8 ? gs8 : gs9, o, lane, go);
  }
  DI void stage(int s, int go = -1) const {
    int lane = __builtin_amdgcn_mbcnt_hi(~0u, __builtin_amdgcn_mbcnt_lo(~0u, 0u));
    asm volatile("" : "+v"(lane));
    stage1(s, 0, lane, go); stage1(s, 1, lane, go); stage1(s, 2, lane, go); stage1(s, 3, lane, go);
  }
  DI u32x4 frag(int s, int n) const { const unsigned a = ra[area(s)][n] + (s & 1) * 4096; return trfrag(a, a + 512); }
  DI void frags(u32x4 (&d)[4], int s) const {
#pragma unroll
    for (int n = 0; n < 4; ++n) d[n] = frag(s, n);
  }
  DI void set_ra(int h, int lane) {
    const int G = lane >> 4;
#pragma unroll
    for (int i = 0; i < 4; ++i) {
      ra[h][i] = lds(4 * h) + ((G * 1024 + ((lane >> 2) & 3) * 128 + ((2 * (G & 1) | ((lane >> 3) & 1)) << 5) + (lane & 3) * 8) ^ (((i + w) & 3) << 5));
      asm volatile("" : "+v"(ra[h][i]));
    }
  }
  DI int aslot(int s) const { return s < 7 ? hks(s) : 74 + so7 + s - 7; }
};

template <bool GLU> DI unsigned epilogue1(P1& c, f32x4 (&acc)[4][2][3], W2& W, u32x4 (&wr)[3][4], int cg, char* ws STAMP_ARGS) {
  int tid = threadIdx.x;
  asm volatile("" : "+v"(tid));
  const int lane = tid & 63, w = __builtin_amdgcn_readfirstlane(tid >> 6), sr = lane & 15, G = lane >> 4;
  const int wn = ((w >> 2) << 1) | (w & 1), wm = (w >> 1) & 1, rb = c.rb, lc01 = c.lc01, lc2 = c.lc2;
  constexpr bool glu = GLU;
  const bool isq = cg >= 13, nt3 = glu || w < 4;
  const unsigned tag = c.tag;
  auto bounce = [&]() {
    W.set_ra(0, lane);
    W.frags(wr[0], 0); W.frags(wr[1], 1);
    wait_vl<63>();
    W.stage(2); W.stage(3);
    SB();
  };
  auto stats_ok = [&]() { return __all(c.gr[0].y == tag && c.gr[1].y == tag && c.gr[2].y == tag && c.gr[3].y == tag && c.gc[0].y == tag && c.gc[1].y == tag); };
  if (!stats_ok()) {
#pragma unroll 1
    for (int spin = 0; spin < SPIN_MAX; ++spin) {
#pragma unroll
      for (int e = 0; e < 6; ++e) ld_stat(c, e);
      asm volatile("" : "+v"(c.gr[0]), "+v"(c.gr[1]), "+v"(c.gr[2]), "+v"(c.gr[3]), "+v"(c.gc[0]), "+v"(c.gc[1]));
      if (stats_ok()) break;
      __builtin_amdgcn_s_sleep(2);
    }
  }
  unsigned cs0 = c.gc[0].x, cs1 = c.gc[1].x;
  asm volatile("" : "+v"(cs0), "+v"(cs1));
  float sdv[4], rs[4], rsl[4], ce[4];
#pragma unroll
  for (int st = 0; st < 4; ++st) {
    const unsigned g = c.gr[st].x;
    sdv[st] = hi16(g); rs[st] = __builtin_amdgcn_rcpf(sdv[st]);
    rsl[st] = 1.44269504f * rs[st]; ce[st] = 1e-6f * sdv[st];
    const unsigned mm = (g & 0xffffu) * 0x10001u;
#pragma unroll
    for (int z = 0; z < 6; ++z)
      acc[st][z / 3][z % 3] = mfma16(u32x4{z < 4 ? cs0 : cs1, 0u, 0u, 0u}, u32x4{G == (z & 3) ? mm : 0u, 0u, 0u, 0u}, acc[st][z / 3][z % 3]);
  }
  STAMP(9);
  const rsrc_t rsw = c.rsw;
  const unsigned actb = W_ACT + (8 * rb + 4 * wm) * NSL * 1024 + lane * 16;
  const unsigned acts = W_ACT + (8 * rb + 4 * wm + (G & 1)) * NSL * 1024 + ((2 * (wn & 1) + (G >> 1)) * 16 + sr) * 16;
  auto store_split = [&](int pair, int ks, u32x2 a, u32x2 b) {
    const auto r0 = __builtin_amdgcn_permlane16_swap(a.x, b.x, false, false), r1 = __builtin_amdgcn_permlane16_swap(a.y, b.y, false, false);
    st128(rsw, acts + (2 * pair * NSL + ks) * 1024, u32x4{r0[0], r1[0], r0[1], r1[1]});
  };
  auto phi4 = [&](int st, f32x4 v) {
    return u32x2{pk2(phi(v[0], rs[st], rsl[st], ce[st]), phi(v[1], rs[st], rsl[st], ce[st])), pk2(phi(v[2], rs[st], rsl[st], ce[st]), phi(v[3], rs[st], rsl[st], ce[st]))};
  };
  auto glu4 = [&](int st, int i) {
    float h[4];
#pragma unroll
    for (int j = 0; j < 4; ++j) {
      const float e = __builtin_amdgcn_exp2f(acc[st][0][i][j] * -rsl[st]);
      h[j] = acc[st][1][i][j] * __builtin_amdgcn_rcpf(__builtin_fmaf(e, sdv[st], sdv[st]));
    }
    return u32x2{pk2(h[0], h[1]), pk2(h[2], h[3])};
  };
  if (glu) {
#pragma unroll
    for (int pair = 0; pair < 2; ++pair) {
#pragma unroll
      for (int st = 2 * pair; st < 2 * pair + 2; ++st) {
        const u32x2 a = glu4(st, 0), b = glu4(st, 1);
        st128(rsw, actb + (st * NSL + 6 * cg + (lc01 >> 5)) * 1024, u32x4{a.x, a.y, b.x, b.y});
      }
      store_split(pair, 6 * cg + (lc2 >> 5), glu4(2 * pair, 2), glu4(2 * pair + 1, 2));
    }
  } else if (isq) {
    const int sp0 = 54 + 5 * (cg - 13);
#pragma unroll
    for (int sd = 0; sd < 2; ++sd)
#pragma unroll
      for (int pair = 0; pair < 2; ++pair) {
#pragma unroll
        for (int st = 2 * pair; st < 2 * pair + 2; ++st) {
          const u32x2 a = phi4(st, acc[st][sd][0]), b = phi4(st, acc[st][sd][1]);
          st128(rsw, actb + (st * NSL + sp0 + 10 * sd + (lc01 >> 5)) * 1024, u32x4{a.x, a.y, b.x, b.y});
        }
        if (nt3) store_split(pair, sp0 + 10 * sd + 2, phi4(2 * pair, acc[2 * pair][sd][2]), phi4(2 * pair + 1, acc[2 * pair + 1][sd][2]));
      }
  } else {
    unsigned ia[3][2];
#pragma unroll
    for (int i = 0; i < 3; ++i) {
      const int u = ((i < 2 ? lc01 + 16 * i : lc2) >> 2) + G;
      ia[i][0] = KVI + (64 * wm + sr) * 640 + ((u ^ sr) << 3); ia[i][1] = KVI + (64 * wm + sr) * 640 + (((u + 40) ^ sr) << 3);
    }
#pragma unroll
    for (int i = 0; i < 3; ++i) {
      if (i == 2 && !nt3) break;
#pragma unroll
      for (int st = 0; st < 4; ++st) {
        const f32x4 v = acc[st][1][i];
        *(LAS u32x2*)(ia[i][0] + st * 10240) = phi4(st, acc[st][0][i]);
        *(LAS u32x2*)(ia[i][1] + st * 10240) = u32x2{pk2(v[0] * rs[st], v[1] * rs[st]), pk2(v[2] * rs[st], v[3] * rs[st])};
      }
    }
    KSTAMP(8);
    wait_bar<63>();
    KSTAMP(10);
    const int hf = lane >> 5, q = (lane >> 2) & 3, p = lane & 3;
    const int uk = min(5 * w + 4 * (G & 1) + p, 39);
    unsigned rk[2], rv[2];
#pragma unroll
    for (int t = 0; t < 2; ++t) {
      const int row = 8 * hf + 4 * t + q;
      rk[t] = KVI + row * 640 + ((uk ^ row) << 3); rv[t] = KVI + row * 640 + (((uk + 40) ^ row) << 3);
    }
    u32x4 kf[8], vf[8];
#pragma unroll
    for (int ks = 0; ks < 8; ++ks) {
      const unsigned hi = ks >= 4 ? 40960 : 0;
      kf[ks] = trfrag(rk[0] + hi + (ks & 3) * 10240, rk[1] + hi + (ks & 3) * 10240);
      vf[ks] = trfrag(rv[0] + hi + (ks & 3) * 10240, rv[1] + hi + (ks & 3) * 10240);
    }
    SB();
    f32x16 kv, kz;
#pragma unroll
    for (int i = 0; i < 16; ++i) { kv[i] = 0.f; kz[i] = 0.f; }
    const bool one = (lane & 31) >= 20;
#pragma unroll
    for (int ks = 0; ks < 8; ++ks) {
#pragma unroll
      for (int i = 0; i < 4; ++i) vf[ks][i] = one ? 0x3f803f80u : vf[ks][i];
      if (ks & 1) kz = mfma32(kf[ks], vf[ks], kz); else kv = mfma32(kf[ks], vf[ks], kv);
    }
    kv += kz;
    KSTAMP(11);
#pragma unroll
    for (int j = 0; j < 3; ++j)
      st128(rsw, ((lane & 31) <= 20 && (j < 2 || hf == 0)) ? W_KVP + (rb * 16 + 4 * (cg - 9) + (w >> 1)) * 4096 + ((w & 1) * 105 + (lane & 31) * 5 + 2 * j + hf) * 16 : 0x80000000u,
            __builtin_bit_cast(u32x4, f32x4{kv[4 * j], kv[4 * j + 1], kv[4 * j + 2], kv[4 * j + 3]}));
  }
  STAMP(3);
  if (!glu && !isq) {
    asm volatile("s_waitcnt vmcnt(0)" ::: "memory");
    if (lane < 2) st_flag(ws + W_FLAGKV + ((4 * (cg - 9) + (w >> 1)) * 2 + lane) * 128 + (16 * (w & 1) + rb) * 4, tag);
  } else asm volatile("s_waitcnt vmcnt(0)" ::: "memory");
  if (glu && w < 4) bounce();
  wait_bar<63>();
  if (tid == 0) st_flag(ws + W_FLAG1 + rb * 128 + cg * 4, tag);
  if (glu && w >= 4) bounce();
  return tag;
}

template <bool GLU> DI unsigned phase1(int rb, int cg, int cgp, W2& W, u32x4 (&wr)[3][4], const u16* x, const u16* wqkv, const u16* wg, const u16* wu, const u16* wd, char* ws STAMP_ARGS) {
  const int tid = threadIdx.x, lane = tid & 63, w = __builtin_amdgcn_readfirstlane(tid >> 6);
  const int wn = ((w >> 2) << 1) | (w & 1), wm = (w >> 1) & 1;
  P1 c;
  const int side = w & 1, oct = w >> 1, rho = lane >> 3, pi = lane & 7;
  c.voffx = (8 * w + rho) * (D * 2) + ((pi ^ (4 * (w & 1) + (rho >> 1))) << 4);
  c.xbase = (const char*)x + (size_t)rb * 128 * D * 2;
  c.wdst = WS + (side * 12 + oct) * 1024; c.xdst = XS + w * 1024;
  issueX(c, 0, 0); issueX(c, 0, 1);
  constexpr bool glu = GLU;
  const bool isq = cg >= 13;
  auto col0 = [&](int sd) { return glu ? 192 * cg : isq ? 160 * (cg - 13) + 320 * sd : 640 + 640 * sd + 160 * (cg - 9); };
  const int shift = (!glu && !(cg & 1)) ? 32 : 0;
  c.wbase = glu ? (side ? (const char*)wu : (const char*)wg) : (const char*)wqkv;
  const int pitch = glu ? F * 2 : 3 * D * 2;
  c.voff = (col0(side) - shift + 16 * ((pi >> 1) ^ (2 * (oct & 1) | ((rho >> 1) & 1)))) * 2 + (8 * oct + rho) * pitch + (pi & 1) * 16;
  c.wstep = 32 * pitch;
  issueW1(c, 0, 0); issueW1(c, 0, 1); issueW1(c, 0, 2);
  STAMP(5);
  SB(); __builtin_amdgcn_s_barrier(); SB();
  issueW1(c, 1, 0); issueW1(c, 1, 1); issueW1(c, 1, 2); issueX(c, 1, 0);
  SB();
  const int sr = lane & 15, G = lane >> 4;
  const int lc01 = 32 * (3 * (wn >> 1) + (wn & 1)), lc2 = 32 * (3 * (wn >> 1) + 2) + 16 * (wn & 1);
  c.xa[0] = XS + (64 * wm + sr) * 128 + ((G ^ (sr >> 1)) << 4); c.xa[1] = c.xa[0] ^ 64;
  {
    const int q = (lane >> 2) & 3, p = lane & 3;
    const unsigned b0 = WS + G * 1024 + q * 128 + p * 8, sw = 2 * (G & 1) | (q >> 1);
#pragma unroll
    for (int i = 0; i < 3; ++i) { const int lc = (i < 2 ? lc01 + 16 * i : lc2) + shift; c.wa[i] = b0 + (lc >> 6) * 4096 + ((((lc >> 4) & 3) ^ sw) << 5); }
  }
  asm volatile("" : "+v"(c.xa[0]), "+v"(c.xa[1]), "+v"(c.wa[0]), "+v"(c.wa[1]), "+v"(c.wa[2]));
  SB();
  issueW1(c, 2, 0); issueW1(c, 2, 1); issueW1(c, 2, 2); issueX(c, 1, 1);
  issueW1(c, 3, 0); issueW1(c, 3, 1); issueW1(c, 3, 2); issueX(c, 2, 0);
  {
    const char* xr = (const char*)x + (size_t)(8 * blockIdx.x + w) * (D * 2);
    gload<0>(c.xrow[0], min(lane, 39) * 32, xr); gload<16>(c.xrow[1], min(lane, 39) * 32, xr);
    gload32(c.epoch, W_EPOCH, ws);
  }
  SB();
  c.rsw = mk_rsrc(ws);
  c.ws = ws; c.early = glu || isq; c.rb = rb; c.lc01 = lc01; c.lc2 = lc2; c.w = w;
  c.cb0 = col0(0) + (glu ? 0 : 2 * F); c.cbd = glu ? F : isq ? 320 : 640;
  c.wd = (const char*)wd + cgp * 128;
  f32x4 acc[4][2][3];
#pragma unroll
  for (int i = 0; i < 24; ++i) acc[i / 6][(i / 3) & 1][i % 3] = f32x4{0.f, 0.f, 0.f, 0.f};
  if (GLU) mainloop<0>(c, acc STAMP_PASS);
  else if (w < 4) {
    c.voff2 = (16 * w + rho) * (D * 2) + ((pi ^ (rho >> 1)) << 4);
    mainloop<1>(c, acc STAMP_PASS);
  } else {
    c.voff2 = (col0(0) - shift + 16 * ((pi >> 1) ^ (2 * (w & 1) | ((rho >> 1) & 1)))) * 2 + (8 * (w - 4) + rho) * pitch + (pi & 1) * 16;
    mainloop<2>(c, acc STAMP_PASS);
  }
  STAMP(2);
  return epilogue1<GLU>(c, acc, W, wr, cg, ws STAMP_PASS);
}

template <class F1> DI void o_share(int P, int rbo, int nrb, unsigned fo, unsigned tag, char* ws, int lane, F1 w1 STAMP_ARGS) {
  const int w = __builtin_amdgcn_readfirstlane(threadIdx.x >> 6), sr = lane & 15, G = lane >> 4;
  const rsrc_t rsw = mk_rsrc(ws);
  const unsigned l16 = lane * 16;
  if (w == 0) poll(lane < 32 ? ws + W_FLAGKV + (P * 2 + ((rbo >> 1) & 1)) * 128 + lane * 4 : ws + W_FLAG1 + (rbo + lane - 32) * 128 + (13 + ((P >> 2) & 1)) * 4,
                   lane < 32 + nrb, tag);
  else w1();
  const int s0 = (40 * P) / 32;
  unsigned ta[8][2];
#pragma unroll
  for (int i = 0; i < 8; ++i) {
    const int b = i >> 2, sp = s0 + ((i >> 1) & 1);
    const int e = (i & 1) == 0 ? sr : (sr & 3) == b ? 20 : ((sr & 2) && (sr >> 3) == b) ? 16 + 2 * ((sr >> 2) & 1) + (sr & 1) : -1;
#pragma unroll
    for (int hl = 0; hl < 2; ++hl) {
      const int d = 32 * sp + (sp % 5 == 2 ? 8 * G + 4 * hl : 16 * hl + 4 * G) - 20 * (2 * P + b);
      ta[i][hl] = (e >= 0 && d >= 0 && d < 20) ? T2 + b * 840 + e * 40 + d * 2 : T2 + 1680;
      asm volatile("" : "+v"(ta[i][hl]));
    }
  }
  if (w == 7) *(LAS u32x2*)(T2 + 1680) = u32x2{0u, 0u};
  wait_bar<63>();
  XSTAMP(10);
  const unsigned tile = W_ACT + (8 * rbo + w) * NSL * 1024;
  u32x4 pqf[3][2], part[16];
  const bool pact = w >= 4 && (w - 4) * 64 + lane < 210;
  if (pact) {
#pragma unroll
    for (int i = 0; i < 16; ++i) part[i] = ld128s(rsw, l16, W_KVP + (i * 16 + P) * 4096 + (w - 4) * 1024);
  }
#pragma unroll
  for (int r = 0; r < 3; ++r)
#pragma unroll
    for (int i = 0; i < 2; ++i) pqf[r][i] = ld128s(rsw, r < 2 || nrb == 3 ? l16 : 0x80000000u, tile + (r * 8 * NSL + 54 + s0 + i) * 1024);
  const unsigned hf = ld32(rsw, W_FLAG1 + fo);
  SB();
  if (pact) {
    f32x4 t[4];
#pragma unroll
    for (int i = 0; i < 4; ++i)
      t[i] = (__builtin_bit_cast(f32x4, part[4 * i]) + __builtin_bit_cast(f32x4, part[4 * i + 1])) + (__builtin_bit_cast(f32x4, part[4 * i + 2]) + __builtin_bit_cast(f32x4, part[4 * i + 3]));
    t[0] = (t[0] + t[1]) + (t[2] + t[3]);
    *(LAS u32x2*)(T2 + ((w - 4) * 64 + lane) * 8) = u32x2{pk2(t[0][0], t[0][1]), pk2(t[0][2], t[0][3])};
  }
  OSTAMP(2);
  wait_bar<63>();
  OSTAMP(3);
  u32x4 a[8];
#pragma unroll
  for (int i = 0; i < 8; ++i) { const u32x2 lo = *(LAS u32x2*)ta[i][0], hi = *(LAS u32x2*)ta[i][1]; a[i] = u32x4{lo.x, lo.y, hi.x, hi.y}; }
#pragma unroll
  for (int r = 0; r < 3; ++r) {
    const f32x4 z = {0.f, 0.f, 0.f, 0.f};
    const f32x4 na = mfma16(a[2], pqf[r][1], mfma16(a[0], pqf[r][0], z)), nb = mfma16(a[6], pqf[r][1], mfma16(a[4], pqf[r][0], z));
    const f32x4 xx = mfma16(a[7], pqf[r][1], mfma16(a[5], pqf[r][0], mfma16(a[3], pqf[r][1], mfma16(a[1], pqf[r][0], z))));
    const float ra = __builtin_amdgcn_rcpf(xx[0] + 1e-6f), rb = __builtin_amdgcn_rcpf(xx[1] + 1e-6f), rl = G < 2 ? ra : rb;
    const unsigned tr = (r < 2 || nrb == 3) ? tile + r * 8 * NSL * 1024 : 0x80000000u;
    st128(rsw, tr + (74 + P) * 1024 + l16, u32x4{pk2(na[0] * ra, na[1] * ra), pk2(na[2] * ra, na[3] * ra), pk2(nb[0] * rb, nb[1] * rb), pk2(nb[2] * rb, nb[3] * rb)});
    st32(rsw, tr + (90 + (P >> 2)) * 1024 + ((P & 3) * 16 + sr) * 16 + 4 * G, pk2(xx[2] * rl, xx[3] * rl));
  }
  OSTAMP(5);
  asm volatile("s_waitcnt vmcnt(0)" ::: "memory");
  if (lane < nrb) st_flag(ws + W_FLAGO + ((8 * (rbo + lane) + w) * 16 + P) * 4, tag);
  SB();
  if (w == 0) w1();
  if (!__all(sr >= 9 || hf == tag)) poll(ws + W_FLAG1 + fo, sr < 9, tag);
}

template <int NS>
DI void tail(W2& W, u32x4 (&wr)[3][4], int st0, int cgp, int P, int jj, int pre, unsigned tag, const u16* x, const u16* wo, u16* out, char* ws STAMP_ARGS) {
  int lane = __builtin_amdgcn_mbcnt_hi(~0u, __builtin_amdgcn_mbcnt_lo(~0u, 0u));
  asm volatile("" : "+v"(lane));
  const int w = W.w, sr = lane & 15;
  constexpr int RW = NS == 6 ? 3 : 2;
  W.init_o(wo, cgp);
  W.set_ra(0, lane); W.set_ra(1, lane);
  const rsrc_t rsw = mk_rsrc(ws);
  const unsigned l16 = lane * 16;
  const unsigned fo = ((lane & 16) ? (st0 + NS - 1) >> 3 : st0 >> 3) * 128 + sr * 4;
  const unsigned mail = 2032 + (w == 0 ? (lane & 7) : w) * 2048;
  if (w == 0) *(LAS unsigned*)mail = ~tag;
  const int rot = w >> 2;
  auto gst = [&](int st) { return st == NS - 1 ? (rot ? 0 : NS - 1) : st + rot; };
  const unsigned abase = W_ACT + st0 * NSL * 1024;
  constexpr int AH = NS == 6 ? 2 : 5;
  u32x4 a[10][NS], wl[4];
  auto issueA1 = [&](int s, int st) { a[s][st] = ld128s(rsw, l16, abase + (W.aslot(s) + gst(st) * NSL) * 1024); };
  auto first = [&]() {
#pragma unroll
    for (int e = 0; e < AH * NS; ++e) issueA1(e / NS, e % NS);
    SB();
  };
  if (NS == 6) {
    W.stage(4); W.stage(5);
    wait_v<8>();
    W.frags(wr[2], 2);
    wait_vl<63>();
    W.stage(6);
    XSTAMP(8);
    if (w == 0) poll(ws + W_FLAG1 + fo, sr < 9, tag);
    wait_bar<63>();
  } else {
    o_share(P, jj < 2 ? 3 * jj : 2 + 2 * jj, jj < 2 ? 3 : 2, fo, tag, ws, lane, [&]() {
      if (pre == 0) { W.stage(0); W.stage(1); }
      W.stage(4); W.stage(5);
      SB();
    } STAMP_PASS);
    XSTAMP(8);
    wait_v<8>();
    W.frags(wr[0], 0); W.frags(wr[1], 1);
    wait_vl<63>();
    W.stage(2); W.stage(3);
  }
  first();
  XSTAMP(11);
  __builtin_amdgcn_s_setprio(3);
  f32x4 acc[NS][4];
  u32x2 oflag;
  const unsigned oo = W_FLAGO + st0 * 64 + min(lane, 8 * NS - 1) * 8;
  auto vm = [&](int s, int e) {
    if (RW == 2 && s == 3 && e == 0) W.stage(6);
    if (s == 4 && e == 0) W.stage(7);
    if (s + AH <= 6 && e < NS) issueA1(s + AH, e);
    if (s == 4 && e == NS) oflag = ld64(rsw, oo);
    if (s == 5 && e == 0) W.stage(8);
  };
  auto pslot = [&](int src, int l) { const int k = l - 1 - (NS > 3 && l > 8) - (NS == 6 && l > 16); return k < 10 ? EAR + src * 10240 + k * 1024 : k < 18 ? LAT + src * 8192 + (k - 10) * 1024 : src * 2048 + (k - 18) * 1024; };
  auto put = [&](int j) {
    const int pn = j / NS, pst = (pn & 1) ? NS - 1 - j % NS : j % NS, l = 4 * pst + pn;
    if (l != 0 && (l != 8 || NS == 3) && (l != 16 || NS != 6) && l < 22) *(LAS f32x4*)(pslot(w, l) + l16) = acc[pst][pn];
  };
  auto slot = [&](int s, const u32x4 (&wc)[4], bool last) {
    if (s == 2) __builtin_amdgcn_s_setprio(2);
    if (s == 4) __builtin_amdgcn_s_setprio(1);
    if (s == 7) __builtin_amdgcn_s_setprio(0);
#pragma unroll
    for (int i = 0; i < 4 * NS; ++i) {
      const int n = i / NS, st = (n & 1) ? NS - 1 - i % NS : i % NS;
      if (s == 0) mfma16_new(acc[st][n], wc[n], a[s][st]); else mfma16_acc(acc[st][n], wc[n], a[s][st]);
      if ((i & 1) == 0) vm(s, i >> 1);
      if (s >= RW - 1 && s <= 7 && s != 6 && i % NS == NS - 1) wl[n] = W.frag(s + 1, n);
      if (last && i >= 8) put(i - 8);
      SB();
    }
    if (last) {
      asm volatile("s_nop 7");
      SB();
#pragma unroll
      for (int j = 4 * NS - 8; j < 4 * NS; ++j) put(j);
    }
  };
  P2STAMP(0); slot(0, wr[0], false);
  P2STAMP(1); slot(1, wr[1], false);
  P2STAMP(2); slot(2, RW == 3 ? wr[2] : wl, false);
  P2STAMP(3); slot(3, wl, false);
  P2STAMP(4); slot(4, wl, false);
  if (RW == 2) wait_v<5>();
  slot(5, wl, false);
  if (!__all(oflag.x == tag && oflag.y == tag)) {
#pragma unroll 1
    for (int spin = 0; spin < SPIN_MAX; ++spin) {
      u32x2 f;
      if (w == 0) f = ld64(rsw, oo); else f.x = f.y = *(volatile LAS unsigned*)mail;
      if (__all(f.x == tag && f.y == tag)) break;
      __builtin_amdgcn_s_sleep(POLL_SLEEP);
    }
    __builtin_amdgcn_fence(__ATOMIC_ACQUIRE, "wavefront");
  }
  if (w == 0) *(volatile LAS unsigned*)mail = tag;
  SB();
  XSTAMP(15);
#pragma unroll
  for (int e = 0; e < 2 * NS; ++e) issueA1(7 + e / NS, e % NS);
  int tz = __builtin_amdgcn_mbcnt_hi(~0u, __builtin_amdgcn_mbcnt_lo(~0u, 0u));
  asm volatile("" : "+v"(tz));
  const unsigned eo = (16 * (st0 + rot) + (tz & 15)) * D + 64 * cgp + 16 * (w & 3) + 4 * (tz >> 4);
  constexpr int NI = (NS + 1) / 2;
  u32x2 xres[NI];
#pragma unroll
  for (int i = 0; i < NI; ++i) xres[i] = *(const GAS u32x2*)(x + (i == NI - 1 && (NS & 1) && rot ? 0 : 32 * i * D) + eo);
  SB();
  u32x4 wx[4];
  W.frags(wx, 7);
  SB();
  if (w < 6) slot(6, wl, false);
  if (rot) {
    W.stage(9);
#pragma unroll
    for (int e = 0; e < NS; ++e) issueA1(9, e);
  }
  XSTAMP(12);
  slot(7, wx, false);
  if (rot) {
    wait_v<0>();
    W.frags(wx, 9);
    slot(9, wx, false);
  }
  slot(8, wl, true);
  XSTAMP(13);
  wait_bar<63>();
  OSTAMP(7);
  auto finish = [&](int i, f32x4 s) {
    const u32x2 o = {pk2(s[0] + lo16(xres[i].x), s[1] + hi16(xres[i].x)), pk2(s[2] + lo16(xres[i].y), s[3] + hi16(xres[i].y))};
    __hip_atomic_store((GAS u64*)(out + 32 * i * D + eo), __builtin_bit_cast(u64, o), RLX_AGENT);
  };
  f32x4 v[NI][8];
#pragma unroll
  for (int j = 1; j < 8; ++j) {
    const int dh = j >> 2, dn = j & 3, sw = 4 * (rot ^ dh) + ((w - dn) & 3);
#pragma unroll
    for (int i = 0; i < NI; ++i) {
      const int l0 = 4 * (dh ? (2 * i + NS - 1) % NS : 2 * i) + dn, l1 = 4 * (dh ? (2 * i + 1) % NS : 2 * i) + dn;
      const int a0 = pslot(sw, min(l0, 21)), a1 = pslot(sw, min(l1, 21));
      v[i][j] = *(LAS f32x4*)((rot ? a1 : a0) + l16);
      if (l0 >= 22 || l1 >= 22) v[i][j] *= (rot ? l1 : l0) >= 22 ? 0.f : 1.f;
    }
  }
  f32x4 sum[NI];
#pragma unroll
  for (int i = 0; i < NI; ++i) {
    sum[i] = ((acc[2 * i][0] + v[i][1]) + (v[i][2] + v[i][3])) + ((v[i][4] + v[i][5]) + (v[i][6] + v[i][7]));
    if (NS == 6 ? i == 1 : (i < NI - 1 || !(NS & 1) || !rot)) finish(i, sum[i]);
  }
  if constexpr (NS == 6) {
    wait_bar<63>();
    *(LAS f32x4*)((w * 2) * 1024 + l16) = acc[5][2];
    *(LAS f32x4*)((w * 2 + 1) * 1024 + l16) = acc[5][3];
    wait_bar<63>();
    const int o4 = rot ? 0 : 4;
    const f32x4 e = *(LAS f32x4*)((((o4 + ((w - 2) & 3)) * 2)) * 1024 + l16) + *(LAS f32x4*)(((o4 + ((w - 3) & 3)) * 2 + 1) * 1024 + l16);
    if (rot) sum[2] += e; else sum[0] += e;
    finish(0, sum[0]); finish(2, sum[2]);
  }
}

__global__ __launch_bounds__(512, 2) void fused(const u16* __restrict__ x, const u16* __restrict__ wqkv, const u16* __restrict__ wg,
                                                const u16* __restrict__ wu, const u16* __restrict__ wd, const u16* __restrict__ wo,
                                                u16* __restrict__ out, char* __restrict__ ws) {
  asm volatile("" : "+s"(x), "+s"(wqkv), "+s"(wg), "+s"(wu), "+s"(wd), "+s"(wo), "+s"(out), "+s"(ws));
  const int xcd = blockIdx.x & 7, idx = blockIdx.x >> 3;
  STAMP_DECL
  STAMP(0);
  unsigned tag;
  const int half = xcd & 1;
  const int i7 = idx / 7, j7 = idx - 7 * i7;
  const int cg = idx >= 28 ? 4 : j7 < 4 ? 5 * half + j7 : j7 < 6 ? 5 + 2 * half + j7 : 13 + half;
  const int rb = 4 * (xcd >> 1) + (idx >= 28 ? 2 * half + idx - 28 : i7);
  const int lr = idx >= 30 ? idx : idx >= 28 ? idx - 12 : j7 < 4 ? 4 * i7 + j7 : j7 < 6 ? 14 + 2 * i7 + j7 : 26 + i7;
  const u64 tb = lr < 8 ? TB0 : lr < 16 ? TB1 : lr < 24 ? TB2 : TB3;
  const int te = (int)(tb >> (8 * (lr & 7))) & 255;
  const int st0 = 32 * (xcd >> 1) + (te & 31), cgp = 5 * half + (te >> 5);
  if (lr < 18) {
    W2 W;
    u32x4 wr[3][4];
    W.init(wd, cgp);
    tag = phase1<true>(rb, cg, cgp, W, wr, x, wqkv, wg, wu, wd, ws STAMP_PASS);
    STAMP(4);
    tail<6>(W, wr, st0, cgp, 0, 0, 2, tag, x, wo, out, ws STAMP_PASS);
  } else {
    W2 W;
    u32x4 wr[3][4];
    W.init(wd, cgp);
    if (idx >= 30) {
      tag = helper(half, 2 * (xcd >> 1) + idx - 30, x, wqkv, wg, wu, wd, wo, ws STAMP_PASS);
      wait_bar<63>();
    } else tag = phase1<false>(rb, cg, cgp, W, wr, x, wqkv, wg, wu, wd, ws STAMP_PASS);
    STAMP(4);
    const int P = 2 * xcd + (lr & 1), jj = lr >= 30 ? 0 : (lr - 16) >> 1;
    if (lr < 26 || lr >= 30) tail<4>(W, wr, st0, cgp, P, jj, 0, tag, x, wo, out, ws STAMP_PASS);
    else tail<3>(W, wr, st0, cgp, P, jj, 1, tag, x, wo, out, ws STAMP_PASS);
  }
  if (blockIdx.x == 255 && threadIdx.x < 64) {
    const int lane = threadIdx.x & 63;
#pragma unroll
    for (int i = 0; i < 4; ++i) {
      poll(ws + W_FLAG1 + (4 * i + (lane >> 4)) * 128 + (lane & 15) * 4, (lane & 15) < 15, tag);
      poll(ws + W_FLAGO + (4 * i + (lane >> 4)) * 512 + (lane & 15) * 4, true, tag);
    }
    if (lane == 0) st_flag(ws + W_EPOCH, tag);
  }
  STAMP(14);
  if (blockIdx.x == PBLOCK && threadIdx.x < 64) {
    const unsigned t0 = __builtin_amdgcn_readlane(st_t, 0), c0 = __builtin_amdgcn_readlane(st_c, 0);
    const unsigned v = PCYC ? (__builtin_amdgcn_readlane(st_c, PROBE) - c0) / 20u : __builtin_amdgcn_readlane(st_t, PROBE) - t0;
    for (int i = 0; i < 4000000; ++i) { if ((unsigned)__builtin_amdgcn_s_memrealtime() - t0 >= 10000u + v) break; __builtin_amdgcn_s_sleep(1); }
  }
}

extern "C" void kernel_launch(void* const* d_in, const int* in_sizes, int n_in, void* d_out, int out_size, void* d_ws, size_t ws_size,
                              hipStream_t stream) {
  const u16* x = (const u16*)d_in[0]; const u16* wqkv = (const u16*)d_in[1]; const u16* wo = (const u16*)d_in[2];
  const u16* wg = (const u16*)d_in[4]; const u16* wu = (const u16*)d_in[5]; const u16* wd = (const u16*)d_in[6];
  (void)hipFuncSetAttribute((const void*)fused, hipFuncAttributeMaxDynamicSharedMemorySize, LDS_BYTES);
  fused<<<256, 512, LDS_BYTES, stream>>>(x, wqkv, wg, wu, wd, wo, (u16*)d_out, (char*)d_ws);
}
```

```cpp
#include <hip/hip_runtime.h>
#include <hip/hip_bf16.h>
#define PF_EARLY 16
#define PF_LOOP 10
#define SPREAD 2
#define PRIO 1
#define WARM 0
#define LEAN 0
#define JOBABL 0
#define ACTMODE 0
#define POLL_SLEEP 4
typedef unsigned short u16;
typedef unsigned long long u64;
typedef short s16x4 __attribute__((ext_vector_type(4)));
typedef __bf16 bf16x2 __attribute__((ext_vector_type(2)));
typedef __bf16 bf16x8 __attribute__((ext_vector_type(8)));
typedef float f32x16 __attribute__((ext_vector_type(16)));
typedef float f32x4 __attribute__((ext_vector_type(4)));
typedef unsigned u32x4 __attribute__((ext_vector_type(4)));
typedef unsigned u32x2 __attribute__((ext_vector_type(2)));
#define LAS __attribute__((address_space(3)))
#define GAS __attribute__((address_space(1)))
#define DI __device__ __forceinline__
#define RLX_AGENT __ATOMIC_RELAXED, __HIP_MEMORY_SCOPE_AGENT

constexpr int S = 2048, D = 640, F = 1728;
constexpr int NSL = 94;
constexpr int XS = 0, WS = 49152, LDS_BYTES = 163840;
constexpr int KVI = 16384;
constexpr int SPIN_MAX = 1 << 14;
constexpr unsigned W_EPOCH = 0, W_FLAG1 = 1024  , W_FLAGO = 81920  ,
                   W_FLAGKV = 4096  ,
                   W_ROWST = 16384, W_COLST = W_ROWST + 2048 * 8, W_ACT = 1 << 20  ,
                   W_KVP = 14 << 20  , W_STAMP = 20 << 20;
constexpr int SPARE = 147456;
constexpr int EAR = 16384, LAT = 98304;
constexpr int T2 = 0;

constexpr u64 TB0 = 0x322c2620120c0600ull, TB1 = 0x806c6660524c4640ull, TB2 = 0x5c583c381c188c86ull, TB3 = 0x96929d9a7d7a7672ull;

DI float rbf(float f) { return (float)(__bf16)f; }
DI unsigned pk2(float a, float b) { bf16x2 v = {(__bf16)a, (__bf16)b}; return __builtin_bit_cast(unsigned, v); }
DI float lo16(unsigned u) { return __uint_as_float(u << 16); }
DI float hi16(unsigned u) { return __uint_as_float(u & 0xffff0000u); }
DI float dot2(unsigned a, unsigned b, float c) { return __builtin_amdgcn_fdot2_f32_bf16(__builtin_bit_cast(bf16x2, a), __builtin_bit_cast(bf16x2, b), c, false); }
DI f32x16 mfma32(u32x4 a, u32x4 b, f32x16 c) { return __builtin_amdgcn_mfma_f32_32x32x16_bf16(__builtin_bit_cast(bf16x8, a), __builtin_bit_cast(bf16x8, b), c, 0, 0, 0); }
DI f32x4 mfma16(u32x4 a, u32x4 b, f32x4 c) { return __builtin_amdgcn_mfma_f32_16x16x32_bf16(__builtin_bit_cast(bf16x8, a), __builtin_bit_cast(bf16x8, b), c, 0, 0, 0); }
DI void mfma16_acc(f32x4& c, u32x4 a, u32x4 b) { asm volatile("v_mfma_f32_16x16x32_bf16 %0, %1, %2, %0" : "+v"(c) : "v"(a), "v"(b)); }
DI void mfma16_new(f32x4& c, u32x4 a, u32x4 b) { asm volatile("v_mfma_f32_16x16x32_bf16 %0, %1, %2, 0" : "=&v"(c) : "v"(a), "v"(b)); }
DI void dma16(unsigned ldsaddr, unsigned voff, const char* sbase) {
  asm volatile("s_mov_b32 m0, %0\n\ts_nop 0\n\tglobal_load_lds_dwordx4 %1, %2" ::"s"(ldsaddr), "v"(voff), "s"(sbase));
}
DI void dma16p(int go, unsigned ldsaddr, unsigned voff, const char* sbase) {
  asm volatile("s_cmp_eq_u32 %3, 0\n\ts_cbranch_scc1 .Lskip%=\n\ts_mov_b32 m0, %0\n\ts_nop 0\n\tglobal_load_lds_dwordx4 %1, %2\n.Lskip%=:"
               ::"s"(ldsaddr), "v"(voff), "s"(sbase), "s"(go) : "scc");
}
DI void aload(u32x4& d, unsigned voff, const char* sbase) { asm volatile("global_load_dwordx4 %0, %1, %2 sc1" : "=v"(d) : "v"(voff), "s"(sbase)); }
DI void gload32(unsigned& d, unsigned voff, const char* sbase) { asm volatile("global_load_dword %0, %1, %2" : "=v"(d) : "v"(voff), "s"(sbase)); }
template <int OFF> DI void gload(u32x4& d, unsigned voff, const char* sbase) { asm volatile("global_load_dwordx4 %0, %1, %2 offset:%3" : "=v"(d) : "v"(voff), "s"(sbase), "i"(OFF)); }
DI void aload32(unsigned& d, unsigned voff, const char* sbase) { asm volatile("global_load_dword %0, %1, %2 sc1" : "=v"(d) : "v"(voff), "s"(sbase)); }
DI void sload_epoch(unsigned& e, const char* ws) { asm volatile("s_load_dword %0, %1, 0x0\n\ts_waitcnt lgkmcnt(0)" : "=s"(e) : "s"(ws)); }
DI u32x2 trread(unsigned addr) { return __builtin_bit_cast(u32x2, __builtin_amdgcn_ds_read_tr16_b64_v4i16((LAS s16x4*)addr)); }
DI u32x4 trfrag(unsigned a0, unsigned a1) { const u32x2 lo = trread(a0), hi = trread(a1); return u32x4{lo.x, lo.y, hi.x, hi.y}; }
DI u32x4 ldsread128(unsigned addr) { return *(LAS u32x4*)addr; }
DI void st_gran(char* p, unsigned tag, unsigned v) { __hip_atomic_store((GAS u64*)p, ((u64)tag << 32) | v, RLX_AGENT); }
DI unsigned ld_flag(const char* p) { return __hip_atomic_load((const GAS unsigned*)p, RLX_AGENT); }
DI void st_flag(char* p, unsigned v) { __hip_atomic_store((GAS unsigned*)p, v, RLX_AGENT); }
typedef __amdgpu_buffer_rsrc_t rsrc_t;
DI rsrc_t mk_rsrc(char* ws) { return __builtin_amdgcn_make_buffer_rsrc(ws, 0, 0x7fffffff, 0x00020000); }
DI void st128(rsrc_t rs, unsigned off, u32x4 v) { __builtin_amdgcn_raw_buffer_store_b128(v, rs, off, 0, 16); }
DI void st32(rsrc_t rs, unsigned off, unsigned v) { __builtin_amdgcn_raw_buffer_store_b32(v, rs, off, 0, 16); }
DI u32x4 ld128(rsrc_t rs, unsigned off) { return __builtin_amdgcn_raw_buffer_load_b128(rs, off, 0, 16); }
DI u32x4 ld128s(rsrc_t rs, unsigned voff, unsigned soff) { return __builtin_amdgcn_raw_buffer_load_b128(rs, voff, soff, 16); }
DI u32x2 ld64(rsrc_t rs, unsigned off) { return __builtin_amdgcn_raw_buffer_load_b64(rs, off, 0, 16); }
DI unsigned ld32(rsrc_t rs, unsigned off) { return __builtin_amdgcn_raw_buffer_load_b32(rs, off, 0, 16); }
DI float exp2_sat(float t) { return __builtin_amdgcn_fmed3f(__builtin_amdgcn_exp2f(t), 0.f, 1.f); }
DI float phi(float a, float rs, float rsl, float ce) { return __builtin_fmaf(fmaxf(a, ce), rs, exp2_sat(a * rsl)); }

#define SB() __builtin_amdgcn_sched_barrier(0)
#define FENCE() asm volatile("" ::: "memory")
#define WCNT(V, L) (((V) & 15) | (7 << 4) | ((L) << 8) | (((V) >> 4) << 14))
template <int N> DI void wait_v() { SB(); FENCE(); __builtin_amdgcn_s_waitcnt(WCNT(N, 15)); FENCE(); SB(); }
template <int N> DI void wait_vl() { SB(); FENCE(); __builtin_amdgcn_s_waitcnt(WCNT(N, 0)); FENCE(); SB(); }
template <int N> DI void wait_bar() { SB(); FENCE(); __builtin_amdgcn_s_waitcnt(WCNT(N, 0)); __builtin_amdgcn_s_barrier(); FENCE(); SB(); }
DI void drain_bar() { SB(); asm volatile("s_waitcnt vmcnt(0) lgkmcnt(0)\n\ts_barrier" ::: "memory"); SB(); }
DI void poll(const char* p, bool act, unsigned tag) {
#pragma unroll 1
  for (int spin = 0; spin < SPIN_MAX; ++spin) {
    if (__all(!act || ld_flag(p) == tag)) break;
    __builtin_amdgcn_s_sleep(POLL_SLEEP);
  }
  __builtin_amdgcn_fence(__ATOMIC_ACQUIRE, "wavefront");
}
#define STAMP(i)
#define STAMP_DECL
#define STAMP_OUT
#define STAMP_ARGS
#define STAMP_PASS

template <int CTRL, int RM> DI float dpp_add(float v) {
  return v + __builtin_bit_cast(float, __builtin_amdgcn_update_dpp(0, __builtin_bit_cast(int, v), CTRL, RM, 0xf, true));
}
DI float wave_sum(float v) {
  v = dpp_add<0x111, 0xf>(v); v = dpp_add<0x112, 0xf>(v); v = dpp_add<0x114, 0xf>(v); v = dpp_add<0x118, 0xf>(v);
  v = dpp_add<0x142, 0xa>(v); return dpp_add<0x143, 0xc>(v);
}
DI void row_sums(const u32x4 (&xr)[2], float& s, float& q) {
  s = 0.f; q = 0.f;
#pragma unroll
  for (int i = 0; i < 8; ++i) { s = dot2(xr[i >> 2][i & 3], 0x3f803f80u, s); q = dot2(xr[i >> 2][i & 3], xr[i >> 2][i & 3], q); }
  const bool on = (threadIdx.x & 63) < 40;
  s = wave_sum(on ? s : 0.f); q = wave_sum(on ? q : 0.f);
}
DI unsigned row_stat(float s, float q) {
  const float m = s * (1.f / D);
  return pk2(-m, sqrtf(rbf(rbf(fmaxf(__builtin_fmaf(-m, m, q * (1.f / D)), 0.f)) + 1e-5f)));
}
DI unsigned helper(int half, int hr, const u16* x, const u16* wqkv, const u16* wg, const u16* wu, const u16* wd, const u16* wo, char* ws STAMP_ARGS) {
  const int tid = threadIdx.x, lane = tid & 63, w = __builtin_amdgcn_readfirstlane(tid >> 6);
  const int sub = lane >> 3, ch = lane & 7;
  unsigned tag;
  sload_epoch(tag, ws);
  {
    u32x4 xr[2];
    const char* xp = (const char*)x + (size_t)(8 * blockIdx.x + w) * (D * 2) + min(lane, 39) * 32;
    xr[0] = *(const GAS u32x4*)xp; xr[1] = *(const GAS u32x4*)(xp + 16);
    float s, q;
    row_sums(xr, s, q);
    if (lane == 63) st_gran(ws + W_ROWST + (8 * blockIdx.x + w) * 8, tag + 1u, row_stat(s, q));
    wait_v<0>();
  }
  if (w < 6) {
    const int ng = 14 - half, n0 = hr + 8 * w, n = n0 < 15 ? n0 + 2 * ng : n0 - 15, m = n0 < 15 + 2 * ng ? n - 2 * ng : 15;
    if (m < 15) {
      const int sl = n < ng ? 14 * half + n : m < 0 ? 27 + 14 * half + n - ng
                   : m < 5 ? 54 + (half ? (m < 2 ? 3 + m : 5 + m) : (m < 3 ? m : 2 + m)) : m < 10 ? 59 + 5 * half + m : 64 + 5 * half + m;
      const char* base = sl < 27 ? (const char*)wg + sl * 128 : sl < 54 ? (const char*)wu + (sl - 27) * 128 : (const char*)wqkv + (sl - 54) * 128;
      const int pitch = sl < 54 ? F * 2 : 3 * D * 2;
      const unsigned reg = w * 24576, vo = sub * pitch + ch * 16;
      auto chunk = [&](int i) {
#pragma unroll
        for (int j = 0; j < 8; ++j) dma16(reg + (i % 3) * 8192 + j * 1024, vo, base + (size_t)((9 - i) * 64 + j * 8) * pitch);
      };
      chunk(0); chunk(1); chunk(2);
      const unsigned ra = reg + (8 * (lane >> 5) + ((lane >> 2) & 3)) * 128 + ((lane >> 4) & 1) * 32 + (lane & 3) * 8;
      const u32x4 ones = {0x3f803f80u, 0x3f803f80u, 0x3f803f80u, 0x3f803f80u};
      f32x16 c[2];
#pragma unroll
      for (int e = 0; e < 16; ++e) { c[0][e] = 0.f; c[1][e] = 0.f; }
#pragma unroll
      for (int i = 0; i < 10; ++i) {
        if (i < 8) wait_v<16>(); else if (i == 8) wait_v<8>(); else wait_v<0>();
#pragma unroll
        for (int ks = 0; ks < 4; ++ks)
#pragma unroll
          for (int t = 0; t < 2; ++t) { const unsigned a = ra + (i % 3) * 8192 + ks * 2048 + t * 64; c[t] = mfma32(ones, trfrag(a, a + 512), c[t]); }
        wait_vl<63>();
        if (i + 3 < 10) chunk(i + 3);
      }
      const float cs = lane >> 5 ? c[1][0] : c[0][0], hi = rbf(cs);
      st_gran(ws + W_COLST + (sl * 64 + lane) * 8, tag + 1u, pk2(hi, cs - hi));
    }
  }
  if (blockIdx.x == 255 && w == 7) {
#pragma unroll 1
    for (int spin = 0; spin < SPIN_MAX; ++spin) {
      __builtin_amdgcn_s_sleep(64);
      bool ok = true;
#pragma unroll
      for (int i = 0; i < 16; ++i) { const u32x4 v = ld128s(mk_rsrc(ws), lane * 16, W_ROWST + i * 1024); ok &= v[1] == tag + 1u && v[3] == tag + 1u; }
      if (__all(ok)) break;
    }
    if (lane == 0) st_flag(ws + W_EPOCH, tag + 1u);
  }
  STAMP(1);
  STAMP(2);
  return tag + 1u;
}

#define MSTAMP(i)
#define XSTAMP(i) STAMP(i)
#define KSTAMP(i)
#define OSTAMP(i)
#define P2STAMP(J)
#define XV(v) v
struct Frags { u32x4 x[4]; u32x4 w[2][3]; };
struct P1 {
  const char *xbase, *wbase, *xall, *wd;
  char* ws;
  unsigned voffx, voff, voff2, wdst, xdst, xa[2], wa[3], rowo, colo[2], tag, epoch;
  int wstep, w, cgp, rb, cb0, cbd, lc01, lc2;
  rsrc_t rsw;
  u32x2 gr[4], gc[2];
  u32x4 xrow[2];
  float rsum, rsq;
};
DI void w2_stage(unsigned dst, const char* src, int hs, int gs, int o, int lane, int go = -1) {
  const int rho = lane >> 3, pi = lane & 7;
  const unsigned v = (hs * (rho >> 2) + (rho & 3)) * (D * 2) + (((pi >> 1) ^ ((rho >> 1) & 1)) << 5) + (pi & 1) * 16;
  if (__builtin_constant_p(go)) dma16(dst + o * 1024, v ^ ((o & 1) << 6), src + o * gs * (D * 2));
  else dma16p(go, dst + o * 1024, v ^ ((o & 1) << 6), src + o * gs * (D * 2));
}
DI void issueX(const P1& c, int v, int half) { dma16(c.xdst + (v % 3) * 16384 + half * 8192, c.voffx, c.xbase + half * 64 * D * 2 + XV(v) * 128); }
DI void issueW1(const P1& c, int j, int pr) { dma16(c.wdst + (j & 3) * 24576 + pr * 4096, c.voff, c.wbase + (size_t)j * c.wstep + pr * 128); }
DI void ld_stat(P1& c, int e) {
  if (e < 4) c.gr[e] = ld64(c.rsw, c.rowo + e * 128);
  else if (e < 6) c.gc[e - 4] = ld64(c.rsw, c.colo[e - 4]);
}
template <int NT> DI void late_setup(P1& c) {
  const int lane = threadIdx.x & 63, sr = lane & 15, G = lane >> 4;
  c.rowo = W_ROWST + (128 * c.rb + 64 * ((threadIdx.x >> 7) & 1) + sr) * 8;
#pragma unroll
  for (int m = 0; m < 2; ++m) {
    const int z = min(4 * m + G, 5), sd = z >= 3, i = sd ? z - 3 : z;
    const int l1 = c.lc01, l2 = NT == 3 ? c.lc2 : l1, lc = i < 2 ? l1 + 16 * i : l2;
    c.colo[m] = W_COLST + (c.cb0 + (sd ? c.cbd : 0) + lc + sr) * 8;
  }
  asm volatile("" : "+v"(c.rowo), "+v"(c.colo[0]), "+v"(c.colo[1]));
}
template <int MODE> DI void mainloop(P1& c, f32x4 (&acc)[4][2][3] STAMP_ARGS) {
  constexpr int NT = MODE == 2 ? 2 : 3;
  auto readX = [&](Frags& f, int t, int st) { f.x[st] = ldsread128(c.xa[t & 1] + ((t >> 1) % 3) * 16384 + st * 2048); };
  auto readW = [&](Frags& f, int t, int n) {
    const unsigned a = c.wa[n % NT] + (t & 1) * 24576 + (n / NT) * 12288;
    f.w[n / NT][n % NT] = trfrag(a, a + 512);
  };
  auto issue = [&](int t, int e) {
    const int v = (t + 5) / 2, half = (t + 1) & 1, j = t + 4;
    if (MODE == 0) {
      if (e < 3) { if (j < 20) issueW1(c, j, e); }
      else if (e == 3) { if (v < 10) issueX(c, v, half); }
    } else if (MODE == 1) {
      if (e < 2 && v < 10) dma16(XS + (v % 3) * 16384 + half * 8192 + (2 * c.w + e) * 1024, c.voff2 ^ (e << 6), c.xbase + (half * 64 + e * 8) * D * 2 + v * 128);
    } else {
      if (e < 6 && j < 20)
        dma16(WS + (j & 3) * 24576 + e * 4096 + (c.w - 4) * 1024, c.voff2, c.wbase + (size_t)j * c.wstep + (e % 3) * 128 + (e / 3) * c.cbd * 2);
    }
  };
  auto step = [&](int t, const Frags& f, Frags& fn) {
    const bool rd = t + 1 < 20;
    if (rd && ((t + 1) & 1) == 0) {
#pragma unroll
      for (int i = 0; i < NT; ++i) { c.wa[i] = ((t + 1) & 2) ? c.wa[i] + 49152 : c.wa[i] - 49152; asm volatile("" : "+v"(c.wa[i])); }
    }
#pragma unroll
    for (int i = 0; i < 8 * NT; ++i) {
      const int n = i >> 2, st = (n & 1) ? 3 - (i & 3) : (i & 3);
      acc[st][n / NT][n % NT] = mfma16(f.w[n / NT][n % NT], f.x[st], acc[st][n / NT][n % NT]);
      if (MODE == 1) { if (i % 8 == 0) issue(t, i >> 3); } else if ((i & 1) == 0) issue(t, i >> 1);
      {
        const int lane = threadIdx.x & 63;
        if (t == 2 && i == 1) {
          asm volatile("" : "+v"(c.epoch), "+v"(c.xrow[0]), "+v"(c.xrow[1]));
          c.tag = __builtin_amdgcn_readfirstlane(c.epoch) + 1u;
          row_sums(c.xrow, c.rsum, c.rsq);
        }
        if (t == 2 && i == 15) {
          const u32x2 g = {row_stat(c.rsum, c.rsq), c.tag};
          __builtin_amdgcn_raw_buffer_store_b64(g, c.rsw, lane == 63 ? W_ROWST + (8 * blockIdx.x + (threadIdx.x >> 6)) * 8 : 0x80000000u, 0, 16);
        }
        if (t == 7 && i == 1) late_setup<NT>(c);
        if (t == 16 && i >= 3 && i < 9) ld_stat(c, i - 3);
        if (t == 18 && i == 0) asm volatile("" : "+v"(c.gr[0]), "+v"(c.gr[1]), "+v"(c.gr[2]), "+v"(c.gr[3]), "+v"(c.gc[0]), "+v"(c.gc[1]));
        if (MODE == 0 && t >= 18 && (i & 1) && i < 8) {
          const int ks = 8 * (t - 18) + c.w;
          const bool split = ks % 3 == 2;
          w2_stage(EAR + c.w * 10240 + (t - 18) * 4096, c.wd + ks * 32 * (D * 2), split ? 4 : 16, split ? 8 : 4, i >> 1, lane);
        }
      }
      if (rd) {
        const int e = NT == 3 ? ((i & 1) ? i >> 1 : -1) : (i < 12 && i % 3 != 2 ? i - i / 3 : -1);
        if (e == 0) readW(fn, t + 1, 0);
        else if (e >= 1 && e <= 4) readX(fn, t + 1, e - 1);
        else if (e >= 5 && e < 4 + 2 * NT) readW(fn, t + 1, e - 4);
      }
      SB();
    }
  };
  Frags f0, f1;
  wait_bar<15>();
  STAMP(1);
#pragma unroll
  for (int i = 0; i < 4; ++i) readX(f0, 0, i);
#pragma unroll
  for (int i = 0; i < 2 * NT; ++i) readW(f0, 0, i);
  constexpr auto cnt = [](int t) {
    return (MODE == 0 ? (t <= 14 ? 4 : t == 15 ? 3 : 0) : MODE == 1 ? (t <= 14 ? 2 : 0) : (t <= 15 ? 6 : 0)) + (t == 2 ? 1 : t == 16 ? 6 : 0);
  };
#define STEP(T, FC, FN) wait_bar<cnt(T - 1) + cnt(T - 2)>(); step(T, FC, FN);
  wait_bar<12>(); step(0, f0, f1);
  wait_bar<7 + cnt(0)>(); step(1, f1, f0);
  STEP(2, f0, f1) STAMP(7); STEP(3, f1, f0) STEP(4, f0, f1)
  STEP(5, f1, f0) STEP(6, f0, f1) STEP(7, f1, f0) STEP(8, f0, f1) STEP(9, f1, f0)
  MSTAMP(8); STEP(10, f0, f1) STEP(11, f1, f0) STEP(12, f0, f1) MSTAMP(10); STEP(13, f1, f0) STEP(14, f0, f1)
  STEP(15, f1, f0) MSTAMP(11); STEP(16, f0, f1) MSTAMP(12);
  STEP(17, f1, f0)
  MSTAMP(13);
  wait_bar<6>();
  MSTAMP(15);
  step(18, f0, f1);
#undef STEP
  wait_vl<63>(); step(19, f1, f0);
}

struct W2 {
  const char *wd, *src7, *src8, *src9;
  int w, so7, gs7, gs8, gs9;
  unsigned ra[2][4];
  DI void init(const u16* wd_, int cgp) {
    w = __builtin_amdgcn_readfirstlane(threadIdx.x >> 6);
    wd = (const char*)wd_ + cgp * 128;
  }
  DI void init_o(const u16* wo_, int cgp) {
    const char* wo = (const char*)wo_ + cgp * 128;
    so7 = w < 4 ? 2 * w : 3 * w - 4;
    auto osrc = [&](int oi) { return wo + (oi >= 16 ? 160 * (oi - 16) + 16 : 40 * oi) * (D * 2); };
    auto ogs = [&](int oi) { return oi >= 16 ? 40 : 4; };
    src7 = osrc(so7); src8 = osrc(so7 + 1); src9 = osrc(so7 + 2);
    gs7 = ogs(so7); gs8 = ogs(so7 + 1); gs9 = ogs(so7 + 2);
    asm volatile("" : "+s"(src7), "+s"(src8), "+s"(src9), "+s"(so7), "+s"(gs7), "+s"(gs8), "+s"(gs9));
  }
  static DI int area(int s) { return (s >> 2) & 1 || s >= 8 ? ((s >> 1) & 1) == 0 : 0; }
  DI unsigned lds(int s) const { return area(s) ? LAT + w * 8192 + (s & 1) * 4096 : EAR + w * 10240 + (s & 1) * 4096; }
  DI int hks(int s) const { return s < 6 ? 8 * s + w : min(48 + w, 53); }
  DI void stage1(int s, int o, int lane, int go) const {
    const bool split = hks(s) % 3 == 2;
    if (s < 7) w2_stage(lds(s), wd + hks(s) * 32 * (D * 2), split ? 4 : 16, split ? 8 : 4, o, lane, go);
    else w2_stage(lds(s), s == 7 ? src7 : s == 8 ? src8 : src9, 20, s == 7 ? gs7 : s == 8 ? gs8 : gs9, o, lane, go);
  }
  DI void stage(int s, int go = -1) const {
    int lane = __builtin_amdgcn_mbcnt_hi(~0u, __builtin_amdgcn_mbcnt_lo(~0u, 0u));
    asm volatile("" : "+v"(lane));
    stage1(s, 0, lane, go); stage1(s, 1, lane, go); stage1(s, 2, lane, go); stage1(s, 3, lane, go);
  }
  DI u32x4 frag(int s, int n) const { const unsigned a = ra[area(s)][n] + (s & 1) * 4096; return trfrag(a, a + 512); }
  DI void frags(u32x4 (&d)[4], int s) const {
#pragma unroll
    for (int n = 0; n < 4; ++n) d[n] = frag(s, n);
  }
  DI void set_ra(int h, int lane) {
    const int G = lane >> 4;
#pragma unroll
    for (int i = 0; i < 4; ++i) {
      ra[h][i] = lds(4 * h) + ((G * 1024 + ((lane >> 2) & 3) * 128 + ((2 * (G & 1) | ((lane >> 3) & 1)) << 5) + (lane & 3) * 8) ^ (((i + w) & 3) << 5));
      asm volatile("" : "+v"(ra[h][i]));
    }
  }
  DI int aslot(int s) const { return s < 7 ? hks(s) : 74 + so7 + s - 7; }
};

template <bool GLU> DI unsigned epilogue1(P1& c, f32x4 (&acc)[4][2][3], W2& W, u32x4 (&wr)[3][4], int cg, char* ws STAMP_ARGS) {
  int tid = threadIdx.x;
  asm volatile("" : "+v"(tid));
  const int lane = tid & 63, w = __builtin_amdgcn_readfirstlane(tid >> 6), sr = lane & 15, G = lane >> 4;
  const int wn = ((w >> 2) << 1) | (w & 1), wm = (w >> 1) & 1, rb = c.rb, lc01 = c.lc01, lc2 = c.lc2;
  constexpr bool glu = GLU;
  const bool isq = cg >= 13, nt3 = glu || w < 4;
  const unsigned tag = c.tag;
  auto bounce = [&]() {
    W.set_ra(0, lane);
    W.frags(wr[0], 0); W.frags(wr[1], 1);
    wait_vl<63>();
    W.stage(2); W.stage(3);
    SB();
  };
  auto stats_ok = [&]() { return __all(c.gr[0].y == tag && c.gr[1].y == tag && c.gr[2].y == tag && c.gr[3].y == tag && c.gc[0].y == tag && c.gc[1].y == tag); };
  if (!stats_ok()) {
#pragma unroll 1
    for (int spin = 0; spin < SPIN_MAX; ++spin) {
#pragma unroll
      for (int e = 0; e < 6; ++e) ld_stat(c, e);
      asm volatile("" : "+v"(c.gr[0]), "+v"(c.gr[1]), "+v"(c.gr[2]), "+v"(c.gr[3]), "+v"(c.gc[0]), "+v"(c.gc[1]));
      if (stats_ok()) break;
      __builtin_amdgcn_s_sleep(2);
    }
  }
  unsigned cs0 = c.gc[0].x, cs1 = c.gc[1].x;
  asm volatile("" : "+v"(cs0), "+v"(cs1));
  float sdv[4], rs[4], rsl[4], ce[4];
#pragma unroll
  for (int st = 0; st < 4; ++st) {
    const unsigned g = c.gr[st].x;
    sdv[st] = hi16(g); rs[st] = __builtin_amdgcn_rcpf(sdv[st]);
    rsl[st] = 1.44269504f * rs[st]; ce[st] = 1e-6f * sdv[st];
    const unsigned mm = (g & 0xffffu) * 0x10001u;
#pragma unroll
    for (int z = 0; z < 6; ++z)
      acc[st][z / 3][z % 3] = mfma16(u32x4{z < 4 ? cs0 : cs1, 0u, 0u, 0u}, u32x4{G == (z & 3) ? mm : 0u, 0u, 0u, 0u}, acc[st][z / 3][z % 3]);
  }
  STAMP(9);
  const rsrc_t rsw = c.rsw;
  const unsigned actb = W_ACT + (8 * rb + 4 * wm) * NSL * 1024 + lane * 16;
  const unsigned acts = W_ACT + (8 * rb + 4 * wm + (G & 1)) * NSL * 1024 + ((2 * (wn & 1) + (G >> 1)) * 16 + sr) * 16;
  auto store_split = [&](int pair, int ks, u32x2 a, u32x2 b) {
    const auto r0 = __builtin_amdgcn_permlane16_swap(a.x, b.x, false, false), r1 = __builtin_amdgcn_permlane16_swap(a.y, b.y, false, false);
    st128(rsw, acts + (2 * pair * NSL + ks) * 1024, u32x4{r0[0], r1[0], r0[1], r1[1]});
  };
  auto phi4 = [&](int st, f32x4 v) {
    return u32x2{pk2(phi(v[0], rs[st], rsl[st], ce[st]), phi(v[1], rs[st], rsl[st], ce[st])), pk2(phi(v[2], rs[st], rsl[st], ce[st]), phi(v[3], rs[st], rsl[st], ce[st]))};
  };
  auto glu4 = [&](int st, int i) {
    float h[4];
#pragma unroll
    for (int j = 0; j < 4; ++j) {
      const float e = __builtin_amdgcn_exp2f(acc[st][0][i][j] * -rsl[st]);
      h[j] = acc[st][1][i][j] * __builtin_amdgcn_rcpf(__builtin_fmaf(e, sdv[st], sdv[st]));
    }
    return u32x2{pk2(h[0], h[1]), pk2(h[2], h[3])};
  };
  if (glu) {
#pragma unroll
    for (int pair = 0; pair < 2; ++pair) {
#pragma unroll
      for (int st = 2 * pair; st < 2 * pair + 2; ++st) {
        const u32x2 a = glu4(st, 0), b = glu4(st, 1);
        st128(rsw, actb + (st * NSL + 6 * cg + (lc01 >> 5)) * 1024, u32x4{a.x, a.y, b.x, b.y});
      }
      store_split(pair, 6 * cg + (lc2 >> 5), glu4(2 * pair, 2), glu4(2 * pair + 1, 2));
    }
  } else if (isq) {
    const int sp0 = 54 + 5 * (cg - 13);
#pragma unroll
    for (int sd = 0; sd < 2; ++sd)
#pragma unroll
      for (int pair = 0; pair < 2; ++pair) {
#pragma unroll
        for (int st = 2 * pair; st < 2 * pair + 2; ++st) {
          const u32x2 a = phi4(st, acc[st][sd][0]), b = phi4(st, acc[st][sd][1]);
          st128(rsw, actb + (st * NSL + sp0 + 10 * sd + (lc01 >> 5)) * 1024, u32x4{a.x, a.y, b.x, b.y});
        }
        if (nt3) store_split(pair, sp0 + 10 * sd + 2, phi4(2 * pair, acc[2 * pair][sd][2]), phi4(2 * pair + 1, acc[2 * pair + 1][sd][2]));
      }
  } else {
    unsigned ia[3][2];
#pragma unroll
    for (int i = 0; i < 3; ++i) {
      const int u = ((i < 2 ? lc01 + 16 * i : lc2) >> 2) + G;
      ia[i][0] = KVI + (64 * wm + sr) * 640 + ((u ^ sr) << 3); ia[i][1] = KVI + (64 * wm + sr) * 640 + (((u + 40) ^ sr) << 3);
    }
#pragma unroll
    for (int i = 0; i < 3; ++i) {
      if (i == 2 && !nt3) break;
#pragma unroll
      for (int st = 0; st < 4; ++st) {
        const f32x4 v = acc[st][1][i];
        *(LAS u32x2*)(ia[i][0] + st * 10240) = phi4(st, acc[st][0][i]);
        *(LAS u32x2*)(ia[i][1] + st * 10240) = u32x2{pk2(v[0] * rs[st], v[1] * rs[st]), pk2(v[2] * rs[st], v[3] * rs[st])};
      }
    }
    KSTAMP(8);
    wait_bar<63>();
    KSTAMP(10);
    const int hf = lane >> 5, q = (lane >> 2) & 3, p = lane & 3;
    const int uk = min(5 * w + 4 * (G & 1) + p, 39);
    unsigned rk[2], rv[2];
#pragma unroll
    for (int t = 0; t < 2; ++t) {
      const int row = 8 * hf + 4 * t + q;
      rk[t] = KVI + row * 640 + ((uk ^ row) << 3); rv[t] = KVI + row * 640 + (((uk + 40) ^ row) << 3);
    }
    u32x4 kf[8], vf[8];
#pragma unroll
    for (int ks = 0; ks < 8; ++ks) {
      const unsigned hi = ks >= 4 ? 40960 : 0;
      kf[ks] = trfrag(rk[0] + hi + (ks & 3) * 10240, rk[1] + hi + (ks & 3) * 10240);
      vf[ks] = trfrag(rv[0] + hi + (ks & 3) * 10240, rv[1] + hi + (ks & 3) * 10240);
    }
    SB();
    f32x16 kv, kz;
#pragma unroll
    for (int i = 0; i < 16; ++i) { kv[i] = 0.f; kz[i] = 0.f; }
    const bool one = (lane & 31) >= 20;
#pragma unroll
    for (int ks = 0; ks < 8; ++ks) {
#pragma unroll
      for (int i = 0; i < 4; ++i) vf[ks][i] = one ? 0x3f803f80u : vf[ks][i];
      if (ks & 1) kz = mfma32(kf[ks], vf[ks], kz); else kv = mfma32(kf[ks], vf[ks], kv);
    }
    kv += kz;
    KSTAMP(11);
#pragma unroll
    for (int j = 0; j < 3; ++j)
      st128(rsw, ((lane & 31) <= 20 && (j < 2 || hf == 0)) ? W_KVP + (rb * 16 + 4 * (cg - 9) + (w >> 1)) * 4096 + ((w & 1) * 105 + (lane & 31) * 5 + 2 * j + hf) * 16 : 0x80000000u,
            __builtin_bit_cast(u32x4, f32x4{kv[4 * j], kv[4 * j + 1], kv[4 * j + 2], kv[4 * j + 3]}));
  }
  STAMP(3);
  if (!glu && !isq) {
    asm volatile("s_waitcnt vmcnt(0)" ::: "memory");
    if (lane < 2) st_flag(ws + W_FLAGKV + ((4 * (cg - 9) + (w >> 1)) * 2 + lane) * 128 + (16 * (w & 1) + rb) * 4, tag);
  } else asm volatile("s_waitcnt vmcnt(0)" ::: "memory");
  if (glu && w < 4) bounce();
  wait_bar<63>();
  if (tid == 0) st_flag(ws + W_FLAG1 + rb * 128 + cg * 4, tag);
  if (glu && w >= 4) bounce();
  return tag;
}

template <bool GLU> DI unsigned phase1(int rb, int cg, int cgp, W2& W, u32x4 (&wr)[3][4], const u16* x, const u16* wqkv, const u16* wg, const u16* wu, const u16* wd, char* ws STAMP_ARGS) {
  const int tid = threadIdx.x, lane = tid & 63, w = __builtin_amdgcn_readfirstlane(tid >> 6);
  const int wn = ((w >> 2) << 1) | (w & 1), wm = (w >> 1) & 1;
  P1 c;
  const int side = w & 1, oct = w >> 1, rho = lane >> 3, pi = lane & 7;
  c.voffx = (8 * w + rho) * (D * 2) + ((pi ^ (4 * (w & 1) + (rho >> 1))) << 4);
  c.xbase = (const char*)x + (size_t)rb * 128 * D * 2;
  c.wdst = WS + (side * 12 + oct) * 1024; c.xdst = XS + w * 1024;
  issueX(c, 0, 0); issueX(c, 0, 1);
  constexpr bool glu = GLU;
  const bool isq = cg >= 13;
  auto col0 = [&](int sd) { return glu ? 192 * cg : isq ? 160 * (cg - 13) + 320 * sd : 640 + 640 * sd + 160 * (cg - 9); };
  const int shift = (!glu && !(cg & 1)) ? 32 : 0;
  c.wbase = glu ? (side ? (const char*)wu : (const char*)wg) : (const char*)wqkv;
  const int pitch = glu ? F * 2 : 3 * D * 2;
  c.voff = (col0(side) - shift + 16 * ((pi >> 1) ^ (2 * (oct & 1) | ((rho >> 1) & 1)))) * 2 + (8 * oct + rho) * pitch + (pi & 1) * 16;
  c.wstep = 32 * pitch;
  issueW1(c, 0, 0); issueW1(c, 0, 1); issueW1(c, 0, 2);
  STAMP(5);
  SB(); __builtin_amdgcn_s_barrier(); SB();
  issueW1(c, 1, 0); issueW1(c, 1, 1); issueW1(c, 1, 2); issueX(c, 1, 0);
  SB();
  const int sr = lane & 15, G = lane >> 4;
  const int lc01 = 32 * (3 * (wn >> 1) + (wn & 1)), lc2 = 32 * (3 * (wn >> 1) + 2) + 16 * (wn & 1);
  c.xa[0] = XS + (64 * wm + sr) * 128 + ((G ^ (sr >> 1)) << 4); c.xa[1] = c.xa[0] ^ 64;
  {
    const int q = (lane >> 2) & 3, p = lane & 3;
    const unsigned b0 = WS + G * 1024 + q * 128 + p * 8, sw = 2 * (G & 1) | (q >> 1);
#pragma unroll
    for (int i = 0; i < 3; ++i) { const int lc = (i < 2 ? lc01 + 16 * i : lc2) + shift; c.wa[i] = b0 + (lc >> 6) * 4096 + ((((lc >> 4) & 3) ^ sw) << 5); }
  }
  asm volatile("" : "+v"(c.xa[0]), "+v"(c.xa[1]), "+v"(c.wa[0]), "+v"(c.wa[1]), "+v"(c.wa[2]));
  SB();
  issueW1(c, 2, 0); issueW1(c, 2, 1); issueW1(c, 2, 2); issueX(c, 1, 1);
  issueW1(c, 3, 0); issueW1(c, 3, 1); issueW1(c, 3, 2); issueX(c, 2, 0);
  {
    const char* xr = (const char*)x + (size_t)(8 * blockIdx.x + w) * (D * 2);
    gload<0>(c.xrow[0], min(lane, 39) * 32, xr); gload<16>(c.xrow[1], min(lane, 39) * 32, xr);
    gload32(c.epoch, W_EPOCH, ws);
  }
  SB();
  c.rsw = mk_rsrc(ws);
  c.ws = ws; c.rb = rb; c.lc01 = lc01; c.lc2 = lc2; c.w = w;
  c.cb0 = col0(0) + (glu ? 0 : 2 * F); c.cbd = glu ? F : isq ? 320 : 640;
  c.wd = (const char*)wd + cgp * 128;
  f32x4 acc[4][2][3];
#pragma unroll
  for (int i = 0; i < 24; ++i) acc[i / 6][(i / 3) & 1][i % 3] = f32x4{0.f, 0.f, 0.f, 0.f};
  if (GLU) mainloop<0>(c, acc STAMP_PASS);
  else if (w < 4) {
    c.voff2 = (16 * w + rho) * (D * 2) + ((pi ^ (rho >> 1)) << 4);
    mainloop<1>(c, acc STAMP_PASS);
  } else {
    c.voff2 = (col0(0) - shift + 16 * ((pi >> 1) ^ (2 * (w & 1) | ((rho >> 1) & 1)))) * 2 + (8 * (w - 4) + rho) * pitch + (pi & 1) * 16;
    mainloop<2>(c, acc STAMP_PASS);
  }
  STAMP(2);
  return epilogue1<GLU>(c, acc, W, wr, cg, ws STAMP_PASS);
}

template <class F1> DI void o_share(int P, int rbo, int nrb, unsigned fo, unsigned tag, char* ws, int lane, F1 w1 STAMP_ARGS) {
  const int w = __builtin_amdgcn_readfirstlane(threadIdx.x >> 6), sr = lane & 15, G = lane >> 4;
  const rsrc_t rsw = mk_rsrc(ws);
  const unsigned l16 = lane * 16;
  if (w == 0) poll(lane < 32 ? ws + W_FLAGKV + (P * 2 + ((rbo >> 1) & 1)) * 128 + lane * 4 : ws + W_FLAG1 + (rbo + lane - 32) * 128 + (13 + ((P >> 2) & 1)) * 4,
                   lane < 32 + nrb, tag);
  else if (!LEAN) w1();
  const int s0 = (40 * P) / 32;
  unsigned ta[8][2];
#pragma unroll
  for (int i = 0; i < 8; ++i) {
    const int b = i >> 2, sp = s0 + ((i >> 1) & 1);
    const int e = (i & 1) == 0 ? sr : (sr & 3) == b ? 20 : ((sr & 2) && (sr >> 3) == b) ? 16 + 2 * ((sr >> 2) & 1) + (sr & 1) : -1;
#pragma unroll
    for (int hl = 0; hl < 2; ++hl) {
      const int d = 32 * sp + (sp % 5 == 2 ? 8 * G + 4 * hl : 16 * hl + 4 * G) - 20 * (2 * P + b);
      ta[i][hl] = (e >= 0 && d >= 0 && d < 20) ? T2 + b * 840 + e * 40 + d * 2 : T2 + 1680;
      asm volatile("" : "+v"(ta[i][hl]));
    }
  }
  if (w == 7) *(LAS u32x2*)(T2 + 1680) = u32x2{0u, 0u};
  wait_bar<63>();
  XSTAMP(10);
  const unsigned tile = W_ACT + (8 * rbo + w) * NSL * 1024;
  u32x4 pqf[3][2], part[16];
  const bool pact = w >= 4 && (w - 4) * 64 + lane < 210;
  if (pact) {
#pragma unroll
    for (int i = 0; i < 16; ++i) part[i] = ld128s(rsw, l16, W_KVP + (i * 16 + P) * 4096 + (w - 4) * 1024);
  }
#pragma unroll
  for (int r = 0; r < 3; ++r)
#pragma unroll
    for (int i = 0; i < 2; ++i) pqf[r][i] = ld128s(rsw, r < 2 || nrb == 3 ? l16 : 0x80000000u, tile + (r * 8 * NSL + 54 + s0 + i) * 1024);
  const unsigned hf = ld32(rsw, W_FLAG1 + fo);
  SB();
  if (pact) {
    f32x4 t[4];
#pragma unroll
    for (int i = 0; i < 4; ++i)
      t[i] = (__builtin_bit_cast(f32x4, part[4 * i]) + __builtin_bit_cast(f32x4, part[4 * i + 1])) + (__builtin_bit_cast(f32x4, part[4 * i + 2]) + __builtin_bit_cast(f32x4, part[4 * i + 3]));
    t[0] = (t[0] + t[1]) + (t[2] + t[3]);
    *(LAS u32x2*)(T2 + ((w - 4) * 64 + lane) * 8) = u32x2{pk2(t[0][0], t[0][1]), pk2(t[0][2], t[0][3])};
  }
  OSTAMP(2);
  wait_bar<63>();
  OSTAMP(3);
  u32x4 a[8];
#pragma unroll
  for (int i = 0; i < 8; ++i) { const u32x2 lo = *(LAS u32x2*)ta[i][0], hi = *(LAS u32x2*)ta[i][1]; a[i] = u32x4{lo.x, lo.y, hi.x, hi.y}; }
#pragma unroll
  for (int r = 0; r < 3; ++r) {
    const f32x4 z = {0.f, 0.f, 0.f, 0.f};
    const f32x4 na = mfma16(a[2], pqf[r][1], mfma16(a[0], pqf[r][0], z)), nb = mfma16(a[6], pqf[r][1], mfma16(a[4], pqf[r][0], z));
    const f32x4 xx = mfma16(a[7], pqf[r][1], mfma16(a[5], pqf[r][0], mfma16(a[3], pqf[r][1], mfma16(a[1], pqf[r][0], z))));
    const float ra = __builtin_amdgcn_rcpf(xx[0] + 1e-6f), rb = __builtin_amdgcn_rcpf(xx[1] + 1e-6f), rl = G < 2 ? ra : rb;
    const unsigned tr = (r < 2 || nrb == 3) ? tile + r * 8 * NSL * 1024 : 0x80000000u;
    st128(rsw, tr + (74 + P) * 1024 + l16, u32x4{pk2(na[0] * ra, na[1] * ra), pk2(na[2] * ra, na[3] * ra), pk2(nb[0] * rb, nb[1] * rb), pk2(nb[2] * rb, nb[3] * rb)});
    st32(rsw, tr + (90 + (P >> 2)) * 1024 + ((P & 3) * 16 + sr) * 16 + 4 * G, pk2(xx[2] * rl, xx[3] * rl));
  }
  OSTAMP(5);
  asm volatile("s_waitcnt vmcnt(0)" ::: "memory");
  if (lane < nrb) st_flag(ws + W_FLAGO + ((8 * (rbo + lane) + w) * 16 + P) * 4, tag);
  SB();
  if (w == 0 || LEAN) w1();
  if (!__all(sr >= 9 || hf == tag)) poll(ws + W_FLAG1 + fo, sr < 9, tag);
}

template <int NS>
DI void tail(W2& W, u32x4 (&wr)[3][4], int st0, int cgp, int P, int jj, int pre, unsigned tag, const u16* x, const u16* wo, u16* out, char* ws STAMP_ARGS) {
  int lane = __builtin_amdgcn_mbcnt_hi(~0u, __builtin_amdgcn_mbcnt_lo(~0u, 0u));
  asm volatile("" : "+v"(lane));
  const int w = W.w, sr = lane & 15;
  constexpr int RW = NS == 6 ? 3 : 2;
  W.init_o(wo, cgp);
  W.set_ra(0, lane); W.set_ra(1, lane);
  const rsrc_t rsw = mk_rsrc(ws);
  const unsigned l16 = lane * 16;
  const unsigned fo = ((lane & 16) ? (st0 + NS - 1) >> 3 : st0 >> 3) * 128 + sr * 4;
  const unsigned mail = 2032 + (w == 0 ? (lane & 7) : w) * 2048;
  if (w == 0) *(LAS unsigned*)mail = ~tag;
  const int rot = w >> 2;
  auto gst = [&](int st) { return st == NS - 1 ? (rot ? 0 : NS - 1) : st + rot; };
  const unsigned abase = W_ACT + st0 * NSL * 1024;
  constexpr int AH = NS == 6 ? 2 : 5;
  u32x4 a[10][NS], wl[4];
  auto issueA1 = [&](int s, int st) { a[s][st] = ld128s(rsw, l16, abase + (W.aslot(s) + gst(st) * NSL) * 1024); };
  auto first = [&]() {
#pragma unroll
    for (int e = 0; e < AH * NS; ++e) issueA1(e / NS, e % NS);
    SB();
  };
  if (NS == 6) {
    W.stage(4); W.stage(5);
    wait_v<8>();
    W.frags(wr[2], 2);
    wait_vl<63>();
    W.stage(6);
    XSTAMP(8);
    if (w == 0) poll(ws + W_FLAG1 + fo, sr < 9, tag);
    wait_bar<63>();
  } else {
    o_share(P, jj < 2 ? 3 * jj : 2 + 2 * jj, jj < 2 ? 3 : 2, fo, tag, ws, lane, [&]() {
      if (pre == 0) { W.stage(0); W.stage(1); }
      W.stage(4); W.stage(5);
      SB();
    } STAMP_PASS);
    XSTAMP(8);
    wait_v<8>();
    W.frags(wr[0], 0); W.frags(wr[1], 1);
    wait_vl<63>();
    W.stage(2); W.stage(3);
  }
  first();
  XSTAMP(11);
  __builtin_amdgcn_s_setprio(3);
  f32x4 acc[NS][4];
  u32x2 oflag;
  const unsigned oo = W_FLAGO + st0 * 64 + min(lane, 8 * NS - 1) * 8;
  auto vm = [&](int s, int e) {
    if (RW == 2 && s == 3 && e == 0) W.stage(6);
    if (s == 4 && e == 0) W.stage(7);
    if (s + AH <= 6 && e < NS) issueA1(s + AH, e);
    if (s == 4 && e == NS) oflag = ld64(rsw, oo);
    if (s == 5 && e == 0) W.stage(8);
  };
  auto pslot = [&](int src, int l) { const int k = l - 1 - (NS > 3 && l > 8) - (NS == 6 && l > 16); return k < 10 ? EAR + src * 10240 + k * 1024 : k < 18 ? LAT + src * 8192 + (k - 10) * 1024 : src * 2048 + (k - 18) * 1024; };
  auto put = [&](int j) {
    const int pn = j / NS, pst = (pn & 1) ? NS - 1 - j % NS : j % NS, l = 4 * pst + pn;
    if (l != 0 && (l != 8 || NS == 3) && (l != 16 || NS != 6) && l < 22) *(LAS f32x4*)(pslot(w, l) + l16) = acc[pst][pn];
  };
  auto slot = [&](int s, const u32x4 (&wc)[4], bool last) {
    if (s == 2) __builtin_amdgcn_s_setprio(2);
    if (s == 4) __builtin_amdgcn_s_setprio(1);
    if (s == 7) __builtin_amdgcn_s_setprio(0);
#pragma unroll
    for (int i = 0; i < 4 * NS; ++i) {
      const int n = i / NS, st = (n & 1) ? NS - 1 - i % NS : i % NS;
      if (s == 0) mfma16_new(acc[st][n], wc[n], a[s][st]); else mfma16_acc(acc[st][n], wc[n], a[s][st]);
      if ((i & 1) == 0) vm(s, i >> 1);
      if (s >= RW - 1 && s <= 7 && s != 6 && i % NS == NS - 1) wl[n] = W.frag(s + 1, n);
      if (last && i >= 8) put(i - 8);
      SB();
    }
    if (last) {
      asm volatile("s_nop 7");
      SB();
#pragma unroll
      for (int j = 4 * NS - 8; j < 4 * NS; ++j) put(j);
    }
  };
  P2STAMP(0); slot(0, wr[0], false);
  P2STAMP(1); slot(1, wr[1], false);
  P2STAMP(2); slot(2, RW == 3 ? wr[2] : wl, false);
  P2STAMP(3); slot(3, wl, false);
  P2STAMP(4); slot(4, wl, false);
  if (RW == 2) wait_v<5>();
  slot(5, wl, false);
  if (!__all(oflag.x == tag && oflag.y == tag)) {
#pragma unroll 1
    for (int spin = 0; spin < SPIN_MAX; ++spin) {
      u32x2 f;
      if (w == 0) f = ld64(rsw, oo); else f.x = f.y = *(volatile LAS unsigned*)mail;
      if (__all(f.x == tag && f.y == tag)) break;
      __builtin_amdgcn_s_sleep(POLL_SLEEP);
    }
    __builtin_amdgcn_fence(__ATOMIC_ACQUIRE, "wavefront");
  }
  if (w == 0) *(volatile LAS unsigned*)mail = tag;
  SB();
  XSTAMP(15);
#pragma unroll
  for (int e = 0; e < 2 * NS; ++e) issueA1(7 + e / NS, e % NS);
  int tz = __builtin_amdgcn_mbcnt_hi(~0u, __builtin_amdgcn_mbcnt_lo(~0u, 0u));
  asm volatile("" : "+v"(tz));
  const unsigned eo = (16 * (st0 + rot) + (tz & 15)) * D + 64 * cgp + 16 * (w & 3) + 4 * (tz >> 4);
  constexpr int NI = (NS + 1) / 2;
  u32x2 xres[NI];
#pragma unroll
  for (int i = 0; i < NI; ++i) xres[i] = *(const GAS u32x2*)(x + (i == NI - 1 && (NS & 1) && rot ? 0 : 32 * i * D) + eo);
  SB();
  u32x4 wx[4];
  W.frags(wx, 7);
  SB();
  if (w < 6) slot(6, wl, false);
  if (rot) {
    W.stage(9);
#pragma unroll
    for (int e = 0; e < NS; ++e) issueA1(9, e);
  }
  XSTAMP(12);
  slot(7, wx, false);
  if (rot) {
    wait_v<0>();
    W.frags(wx, 9);
    slot(9, wx, false);
  }
  slot(8, wl, true);
  XSTAMP(13);
  wait_bar<63>();
  OSTAMP(7);
  auto finish = [&](int i, f32x4 s) {
    const u32x2 o = {pk2(s[0] + lo16(xres[i].x), s[1] + hi16(xres[i].x)), pk2(s[2] + lo16(xres[i].y), s[3] + hi16(xres[i].y))};
    __hip_atomic_store((GAS u64*)(out + 32 * i * D + eo), __builtin_bit_cast(u64, o), RLX_AGENT);
  };
  f32x4 v[NI][8];
#pragma unroll
  for (int j = 1; j < 8; ++j) {
    const int dh = j >> 2, dn = j & 3, sw = 4 * (rot ^ dh) + ((w - dn) & 3);
#pragma unroll
    for (int i = 0; i < NI; ++i) {
      const int l0 = 4 * (dh ? (2 * i + NS - 1) % NS : 2 * i) + dn, l1 = 4 * (dh ? (2 * i + 1) % NS : 2 * i) + dn;
      const int a0 = pslot(sw, min(l0, 21)), a1 = pslot(sw, min(l1, 21));
      v[i][j] = *(LAS f32x4*)((rot ? a1 : a0) + l16);
      if (l0 >= 22 || l1 >= 22) v[i][j] *= (rot ? l1 : l0) >= 22 ? 0.f : 1.f;
    }
  }
  f32x4 sum[NI];
#pragma unroll
  for (int i = 0; i < NI; ++i) {
    sum[i] = ((acc[2 * i][0] + v[i][1]) + (v[i][2] + v[i][3])) + ((v[i][4] + v[i][5]) + (v[i][6] + v[i][7]));
    if (NS == 6 ? i == 1 : (i < NI - 1 || !(NS & 1) || !rot)) finish(i, sum[i]);
  }
  if constexpr (NS == 6) {
    wait_bar<63>();
    *(LAS f32x4*)((w * 2) * 1024 + l16) = acc[5][2];
    *(LAS f32x4*)((w * 2 + 1) * 1024 + l16) = acc[5][3];
    wait_bar<63>();
    const int o4 = rot ? 0 : 4;
    const f32x4 e = *(LAS f32x4*)((((o4 + ((w - 2) & 3)) * 2)) * 1024 + l16) + *(LAS f32x4*)(((o4 + ((w - 3) & 3)) * 2 + 1) * 1024 + l16);
    if (rot) sum[2] += e; else sum[0] += e;
    finish(0, sum[0]); finish(2, sum[2]);
  }
}

__global__ __launch_bounds__(512, 2) void fused(const u16* __restrict__ x, const u16* __restrict__ wqkv, const u16* __restrict__ wg,
                                                const u16* __restrict__ wu, const u16* __restrict__ wd, const u16* __restrict__ wo,
                                                u16* __restrict__ out, char* __restrict__ ws) {
  asm volatile("" : "+s"(x), "+s"(wqkv), "+s"(wg), "+s"(wu), "+s"(wd), "+s"(wo), "+s"(out), "+s"(ws));
  const int xcd = blockIdx.x & 7, idx = blockIdx.x >> 3;
  STAMP_DECL
  STAMP(0);
  unsigned tag;
  const int half = xcd & 1;
  const int i7 = idx / 7, j7 = idx - 7 * i7;
  const int cg = idx >= 28 ? 4 : j7 < 4 ? 5 * half + j7 : j7 < 6 ? 5 + 2 * half + j7 : 13 + half;
  const int rb = 4 * (xcd >> 1) + (idx >= 28 ? 2 * half + idx - 28 : i7);
  const int lr = idx >= 30 ? idx : idx >= 28 ? idx - 12 : j7 < 4 ? 4 * i7 + j7 : j7 < 6 ? 14 + 2 * i7 + j7 : 26 + i7;
  const u64 tb = lr < 8 ? TB0 : lr < 16 ? TB1 : lr < 24 ? TB2 : TB3;
  const int te = (int)(tb >> (8 * (lr & 7))) & 255;
  const int st0 = 32 * (xcd >> 1) + (te & 31), cgp = 5 * half + (te >> 5);
  if (lr < 18) {
    W2 W;
    u32x4 wr[3][4];
    W.init(wd, cgp);
    tag = phase1<true>(rb, cg, cgp, W, wr, x, wqkv, wg, wu, wd, ws STAMP_PASS);
    STAMP(4);
    tail<6>(W, wr, st0, cgp, 0, 0, 2, tag, x, wo, out, ws STAMP_PASS);
  } else {
    W2 W;
    u32x4 wr[3][4];
    W.init(wd, cgp);
    if (idx >= 30) {
      tag = helper(half, 2 * (xcd >> 1) + idx - 30, x, wqkv, wg, wu, wd, wo, ws STAMP_PASS);
      wait_bar<63>();
    } else tag = phase1<false>(rb, cg, cgp, W, wr, x, wqkv, wg, wu, wd, ws STAMP_PASS);
    STAMP(4);
    const int P = 2 * xcd + (lr & 1), jj = lr >= 30 ? 0 : (lr - 16) >> 1;
    if (lr < 26 || lr >= 30) tail<4>(W, wr, st0, cgp, P, jj, 0, tag, x, wo, out, ws STAMP_PASS);
    else tail<3>(W, wr, st0, cgp, P, jj, 0, tag, x, wo, out, ws STAMP_PASS);
  }
  STAMP(14);
  STAMP_OUT
}

extern "C" void kernel_launch(void* const* d_in, const int* in_sizes, int n_in, void* d_out, int out_size, void* d_ws, size_t ws_size,
                              hipStream_t stream) {
  const u16* x = (const u16*)d_in[0]; const u16* wqkv = (const u16*)d_in[1]; const u16* wo = (const u16*)d_in[2];
  const u16* wg = (const u16*)d_in[4]; const u16* wu = (const u16*)d_in[5]; const u16* wd = (const u16*)d_in[6];
  (void)hipFuncSetAttribute((const void*)fused, hipFuncAttributeMaxDynamicSharedMemorySize, LDS_BYTES);
  fused<<<256, 512, LDS_BYTES, stream>>>(x, wqkv, wg, wu, wd, wo, (u16*)d_out, (char*)d_ws + ((ws_size - (24u << 20)) & ~(size_t)0xfffff));
}
```

```cpp
#define STAMPS
#define NSTAMP 16
#define PBLOCK 8
#define PROBE 0
#define PCYC 0
#include <hip/hip_runtime.h>
#include <hip/hip_bf16.h>
#define PF_EARLY 16
#define PF_LOOP 10
#define SPREAD 2
#define PRIO 1
#define WARM 1
#define JOBABL 0
#define ACTMODE 0
#define POLL_SLEEP 4
typedef unsigned short u16;
typedef unsigned long long u64;
typedef short s16x4 __attribute__((ext_vector_type(4)));
typedef __bf16 bf16x2 __attribute__((ext_vector_type(2)));
typedef __bf16 bf16x8 __attribute__((ext_vector_type(8)));
typedef float f32x16 __attribute__((ext_vector_type(16)));
typedef float f32x4 __attribute__((ext_vector_type(4)));
typedef unsigned u32x4 __attribute__((ext_vector_type(4)));
typedef unsigned u32x2 __attribute__((ext_vector_type(2)));
#define LAS __attribute__((address_space(3)))
#define GAS __attribute__((address_space(1)))
#define DI __device__ __forceinline__
#define RLX_AGENT __ATOMIC_RELAXED, __HIP_MEMORY_SCOPE_AGENT

constexpr int S = 2048, D = 640, F = 1728;
constexpr int NSL = 94;
constexpr int XS = 0, WS = 49152, LDS_BYTES = 163840;
constexpr int KVI = 16384;
constexpr int SPIN_MAX = 1 << 14;
constexpr unsigned W_EPOCH = 0, W_FLAG1 = 1024  , W_FLAGO = 81920  ,
                   W_FLAGKV = 4096  ,
                   W_ROWST = 16384, W_COLST = W_ROWST + 2048 * 8, W_ACT = 1 << 20  ,
                   W_KVP = 14 << 20  , W_STAMP = 20 << 20;
constexpr int SPARE = 147456;
constexpr int EAR = 16384, LAT = 98304;
constexpr int T2 = 0;

constexpr u64 TB0 = 0x322c2620120c0600ull, TB1 = 0x806c6660524c4640ull, TB2 = 0x5c583c381c188c86ull, TB3 = 0x96929d9a7d7a7672ull;

DI float rbf(float f) { return (float)(__bf16)f; }
DI unsigned pk2(float a, float b) { bf16x2 v = {(__bf16)a, (__bf16)b}; return __builtin_bit_cast(unsigned, v); }
DI float lo16(unsigned u) { return __uint_as_float(u << 16); }
DI float hi16(unsigned u) { return __uint_as_float(u & 0xffff0000u); }
DI float dot2(unsigned a, unsigned b, float c) { return __builtin_amdgcn_fdot2_f32_bf16(__builtin_bit_cast(bf16x2, a), __builtin_bit_cast(bf16x2, b), c, false); }
DI f32x16 mfma32(u32x4 a, u32x4 b, f32x16 c) { return __builtin_amdgcn_mfma_f32_32x32x16_bf16(__builtin_bit_cast(bf16x8, a), __builtin_bit_cast(bf16x8, b), c, 0, 0, 0); }
DI f32x4 mfma16(u32x4 a, u32x4 b, f32x4 c) { return __builtin_amdgcn_mfma_f32_16x16x32_bf16(__builtin_bit_cast(bf16x8, a), __builtin_bit_cast(bf16x8, b), c, 0, 0, 0); }
DI void mfma16_acc(f32x4& c, u32x4 a, u32x4 b) { asm volatile("v_mfma_f32_16x16x32_bf16 %0, %1, %2, %0" : "+v"(c) : "v"(a), "v"(b)); }
DI void mfma16_new(f32x4& c, u32x4 a, u32x4 b) { asm volatile("v_mfma_f32_16x16x32_bf16 %0, %1, %2, 0" : "=&v"(c) : "v"(a), "v"(b)); }
DI void dma16(unsigned ldsaddr, unsigned voff, const char* sbase) {
  asm volatile("s_mov_b32 m0, %0\n\ts_nop 0\n\tglobal_load_lds_dwordx4 %1, %2" ::"s"(ldsaddr), "v"(voff), "s"(sbase));
}
DI void dma16p(int go, unsigned ldsaddr, unsigned voff, const char* sbase) {
  asm volatile("s_cmp_eq_u32 %3, 0\n\ts_cbranch_scc1 .Lskip%=\n\ts_mov_b32 m0, %0\n\ts_nop 0\n\tglobal_load_lds_dwordx4 %1, %2\n.Lskip%=:"
               ::"s"(ldsaddr), "v"(voff), "s"(sbase), "s"(go) : "scc");
}
DI void aload(u32x4& d, unsigned voff, const char* sbase) { asm volatile("global_load_dwordx4 %0, %1, %2 sc1" : "=v"(d) : "v"(voff), "s"(sbase)); }
DI void gload32(unsigned& d, unsigned voff, const char* sbase) { asm volatile("global_load_dword %0, %1, %2" : "=v"(d) : "v"(voff), "s"(sbase)); }
template <int OFF> DI void gload(u32x4& d, unsigned voff, const char* sbase) { asm volatile("global_load_dwordx4 %0, %1, %2 offset:%3" : "=v"(d) : "v"(voff), "s"(sbase), "i"(OFF)); }
DI void aload32(unsigned& d, unsigned voff, const char* sbase) { asm volatile("global_load_dword %0, %1, %2 sc1" : "=v"(d) : "v"(voff), "s"(sbase)); }
DI void sload_epoch(unsigned& e, const char* ws) { asm volatile("s_load_dword %0, %1, 0x0\n\ts_waitcnt lgkmcnt(0)" : "=s"(e) : "s"(ws)); }
DI u32x2 trread(unsigned addr) { return __builtin_bit_cast(u32x2, __builtin_amdgcn_ds_read_tr16_b64_v4i16((LAS s16x4*)addr)); }
DI u32x4 trfrag(unsigned a0, unsigned a1) { const u32x2 lo = trread(a0), hi = trread(a1); return u32x4{lo.x, lo.y, hi.x, hi.y}; }
DI u32x4 ldsread128(unsigned addr) { return *(LAS u32x4*)addr; }
DI void st_gran(char* p, unsigned tag, unsigned v) { __hip_atomic_store((GAS u64*)p, ((u64)tag << 32) | v, RLX_AGENT); }
DI unsigned ld_flag(const char* p) { return __hip_atomic_load((const GAS unsigned*)p, RLX_AGENT); }
DI void st_flag(char* p, unsigned v) { __hip_atomic_store((GAS unsigned*)p, v, RLX_AGENT); }
typedef __amdgpu_buffer_rsrc_t rsrc_t;
DI rsrc_t mk_rsrc(char* ws) { return __builtin_amdgcn_make_buffer_rsrc(ws, 0, 0x7fffffff, 0x00020000); }
DI void st128(rsrc_t rs, unsigned off, u32x4 v) { __builtin_amdgcn_raw_buffer_store_b128(v, rs, off, 0, 16); }
DI void st32(rsrc_t rs, unsigned off, unsigned v) { __builtin_amdgcn_raw_buffer_store_b32(v, rs, off, 0, 16); }
DI u32x4 ld128(rsrc_t rs, unsigned off) { return __builtin_amdgcn_raw_buffer_load_b128(rs, off, 0, 16); }
DI u32x4 ld128s(rsrc_t rs, unsigned voff, unsigned soff) { return __builtin_amdgcn_raw_buffer_load_b128(rs, voff, soff, 16); }
DI u32x2 ld64(rsrc_t rs, unsigned off) { return __builtin_amdgcn_raw_buffer_load_b64(rs, off, 0, 16); }
DI unsigned ld32(rsrc_t rs, unsigned off) { return __builtin_amdgcn_raw_buffer_load_b32(rs, off, 0, 16); }
DI float exp2_sat(float t) { return __builtin_amdgcn_fmed3f(__builtin_amdgcn_exp2f(t), 0.f, 1.f); }
DI float phi(float a, float rs, float rsl, float ce) { return __builtin_fmaf(fmaxf(a, ce), rs, exp2_sat(a * rsl)); }

#define SB() __builtin_amdgcn_sched_barrier(0)
#define FENCE() asm volatile("" ::: "memory")
#define WCNT(V, L) (((V) & 15) | (7 << 4) | ((L) << 8) | (((V) >> 4) << 14))
template <int N> DI void wait_v() { SB(); FENCE(); __builtin_amdgcn_s_waitcnt(WCNT(N, 15)); FENCE(); SB(); }
template <int N> DI void wait_vl() { SB(); FENCE(); __builtin_amdgcn_s_waitcnt(WCNT(N, 0)); FENCE(); SB(); }
template <int N> DI void wait_bar() { SB(); FENCE(); __builtin_amdgcn_s_waitcnt(WCNT(N, 0)); __builtin_amdgcn_s_barrier(); FENCE(); SB(); }
DI void drain_bar() { SB(); asm volatile("s_waitcnt vmcnt(0) lgkmcnt(0)\n\ts_barrier" ::: "memory"); SB(); }
DI void poll(const char* p, bool act, unsigned tag) {
#pragma unroll 1
  for (int spin = 0; spin < SPIN_MAX; ++spin) {
    if (__all(!act || ld_flag(p) == tag)) break;
    __builtin_amdgcn_s_sleep(POLL_SLEEP);
  }
  __builtin_amdgcn_fence(__ATOMIC_ACQUIRE, "wavefront");
}
#define STAMP(i) do{ SB(); u64 t_, c_; asm volatile("s_memrealtime %0\n\ts_memtime %1\n\ts_waitcnt lgkmcnt(0)":"=s"(t_),"=s"(c_)::"memory"); \
  asm volatile("v_writelane_b32 %0, %2, %4\n\tv_writelane_b32 %1, %3, %4" : "+v"(st_t), "+v"(st_c) : "s"((unsigned)t_), "s"((unsigned)c_), "i"(i)); SB();}while(0)
#define STAMP_DECL unsigned st_t = 0, st_c = 0;
#define STAMP_OUT if((threadIdx.x & 63) < 16){ unsigned* stamps = (unsigned*)(ws + W_STAMP); stamps[((blockIdx.x*8+w)*16+(threadIdx.x&63))*2]=st_t; stamps[((blockIdx.x*8+w)*16+(threadIdx.x&63))*2+1]=st_c; }
#define STAMP_ARGS , unsigned& st_t, unsigned& st_c
#define STAMP_PASS , st_t, st_c

template <int CTRL, int RM> DI float dpp_add(float v) {
  return v + __builtin_bit_cast(float, __builtin_amdgcn_update_dpp(0, __builtin_bit_cast(int, v), CTRL, RM, 0xf, true));
}
DI float wave_sum(float v) {
  v = dpp_add<0x111, 0xf>(v); v = dpp_add<0x112, 0xf>(v); v = dpp_add<0x114, 0xf>(v); v = dpp_add<0x118, 0xf>(v);
  v = dpp_add<0x142, 0xa>(v); return dpp_add<0x143, 0xc>(v);
}
DI void row_sums(const u32x4 (&xr)[2], float& s, float& q) {
  s = 0.f; q = 0.f;
#pragma unroll
  for (int i = 0; i < 8; ++i) { s = dot2(xr[i >> 2][i & 3], 0x3f803f80u, s); q = dot2(xr[i >> 2][i & 3], xr[i >> 2][i & 3], q); }
  const bool on = (threadIdx.x & 63) < 40;
  s = wave_sum(on ? s : 0.f); q = wave_sum(on ? q : 0.f);
}
DI unsigned row_stat(float s, float q) {
  const float m = s * (1.f / D);
  return pk2(-m, sqrtf(rbf(rbf(fmaxf(__builtin_fmaf(-m, m, q * (1.f / D)), 0.f)) + 1e-5f)));
}
DI unsigned helper(int half, int hr, const u16* x, const u16* wqkv, const u16* wg, const u16* wu, const u16* wd, const u16* wo, char* ws STAMP_ARGS) {
  const int tid = threadIdx.x, lane = tid & 63, w = __builtin_amdgcn_readfirstlane(tid >> 6);
  const int sub = lane >> 3, ch = lane & 7;
  unsigned tag;
  sload_epoch(tag, ws);
  {
    u32x4 xr[2];
    const char* xp = (const char*)x + (size_t)(8 * blockIdx.x + w) * (D * 2) + min(lane, 39) * 32;
    xr[0] = *(const GAS u32x4*)xp; xr[1] = *(const GAS u32x4*)(xp + 16);
    float s, q;
    row_sums(xr, s, q);
    if (lane == 63) st_gran(ws + W_ROWST + (8 * blockIdx.x + w) * 8, tag + 1u, row_stat(s, q));
    wait_v<0>();
  }
  if (w < 6) {
    const int n = hr + 8 * w, ng = 14 - half, m = n - 2 * ng;
    if (m < 15) {
      const int sl = n < ng ? 14 * half + n : m < 0 ? 27 + 14 * half + n - ng
                   : m < 5 ? 54 + (half ? (m < 2 ? 3 + m : 5 + m) : (m < 3 ? m : 2 + m)) : m < 10 ? 59 + 5 * half + m : 64 + 5 * half + m;
      const char* base = sl < 27 ? (const char*)wg + sl * 128 : sl < 54 ? (const char*)wu + (sl - 27) * 128 : (const char*)wqkv + (sl - 54) * 128;
      const int pitch = sl < 54 ? F * 2 : 3 * D * 2;
      const unsigned reg = w * 24576, vo = sub * pitch + ch * 16;
      auto chunk = [&](int i) {
#pragma unroll
        for (int j = 0; j < 8; ++j) dma16(reg + (i % 3) * 8192 + j * 1024, vo, base + (size_t)((9 - i) * 64 + j * 8) * pitch);
      };
      chunk(0); chunk(1); chunk(2);
      const unsigned ra = reg + (8 * (lane >> 5) + ((lane >> 2) & 3)) * 128 + ((lane >> 4) & 1) * 32 + (lane & 3) * 8;
      const u32x4 ones = {0x3f803f80u, 0x3f803f80u, 0x3f803f80u, 0x3f803f80u};
      f32x16 c[2];
#pragma unroll
      for (int e = 0; e < 16; ++e) { c[0][e] = 0.f; c[1][e] = 0.f; }
#pragma unroll
      for (int i = 0; i < 10; ++i) {
        if (i < 8) wait_v<16>(); else if (i == 8) wait_v<8>(); else wait_v<0>();
#pragma unroll
        for (int ks = 0; ks < 4; ++ks)
#pragma unroll
          for (int t = 0; t < 2; ++t) { const unsigned a = ra + (i % 3) * 8192 + ks * 2048 + t * 64; c[t] = mfma32(ones, trfrag(a, a + 512), c[t]); }
        wait_vl<63>();
        if (i + 3 < 10) chunk(i + 3);
      }
      const float cs = lane >> 5 ? c[1][0] : c[0][0], hi = rbf(cs);
      st_gran(ws + W_COLST + (sl * 64 + lane) * 8, tag + 1u, pk2(hi, cs - hi));
    }
  }
  STAMP(1);
  if (w >= 6) { __builtin_amdgcn_s_sleep(127); __builtin_amdgcn_s_sleep(127); }
  {
    const int b0 = ((half * 8 + hr) * 8 + w) * 24 * 1024;
#pragma unroll 1
    for (int i = 0; i < 24; ++i) {
      const int b = min(b0 + i * 1024, (F + D) * D * 2 - 1024);
      dma16(SPARE + w * 2048, lane * 16, b < F * D * 2 ? (const char*)wd + b : (const char*)wo + (b - F * D * 2));
    }
    wait_v<0>();
  }
  STAMP(2);
  return tag + 1u;
}

#define MSTAMP(i)
#define XSTAMP(i) STAMP(i)
#define KSTAMP(i)
#define OSTAMP(i)
#define P2STAMP(J)
#define XV(v) v
struct Frags { u32x4 x[4]; u32x4 w[2][3]; };
struct P1 {
  const char *xbase, *wbase, *xall, *wd;
  char* ws;
  unsigned voffx, voff, voff2, wdst, xdst, xa[2], wa[3], rowo, colo[2], tag, epoch;
  int wstep, w, cgp, rb, cb0, cbd, lc01, lc2;
  rsrc_t rsw;
  u32x2 gr[4], gc[2];
  u32x4 xrow[2];
  float rsum, rsq;
  bool early;
};
DI void w2_stage(unsigned dst, const char* src, int hs, int gs, int o, int lane, int go = -1) {
  const int rho = lane >> 3, pi = lane & 7;
  const unsigned v = (hs * (rho >> 2) + (rho & 3)) * (D * 2) + (((pi >> 1) ^ ((rho >> 1) & 1)) << 5) + (pi & 1) * 16;
  if (__builtin_constant_p(go)) dma16(dst + o * 1024, v ^ ((o & 1) << 6), src + o * gs * (D * 2));
  else dma16p(go, dst + o * 1024, v ^ ((o & 1) << 6), src + o * gs * (D * 2));
}
DI void issueX(const P1& c, int v, int half) { dma16(c.xdst + (v % 3) * 16384 + half * 8192, c.voffx, c.xbase + half * 64 * D * 2 + XV(v) * 128); }
DI void issueW1(const P1& c, int j, int pr) { dma16(c.wdst + (j & 3) * 24576 + pr * 4096, c.voff, c.wbase + (size_t)j * c.wstep + pr * 128); }
DI void ld_stat(P1& c, int e) {
  if (e < 4) c.gr[e] = ld64(c.rsw, c.rowo + e * 128);
  else if (e < 6) c.gc[e - 4] = ld64(c.rsw, c.colo[e - 4]);
}
template <int NT> DI void late_setup(P1& c) {
  const int lane = threadIdx.x & 63, sr = lane & 15, G = lane >> 4;
  c.rowo = W_ROWST + (128 * c.rb + 64 * ((threadIdx.x >> 7) & 1) + sr) * 8;
#pragma unroll
  for (int m = 0; m < 2; ++m) {
    const int z = min(4 * m + G, 5), sd = z >= 3, i = sd ? z - 3 : z;
    const int l1 = c.lc01, l2 = NT == 3 ? c.lc2 : l1, lc = i < 2 ? l1 + 16 * i : l2;
    c.colo[m] = W_COLST + (c.cb0 + (sd ? c.cbd : 0) + lc + sr) * 8;
  }
  asm volatile("" : "+v"(c.rowo), "+v"(c.colo[0]), "+v"(c.colo[1]));
}
template <int MODE> DI void mainloop(P1& c, f32x4 (&acc)[4][2][3] STAMP_ARGS) {
  constexpr int NT = MODE == 2 ? 2 : 3;
  auto readX = [&](Frags& f, int t, int st) { f.x[st] = ldsread128(c.xa[t & 1] + ((t >> 1) % 3) * 16384 + st * 2048); };
  auto readW = [&](Frags& f, int t, int n) {
    const unsigned a = c.wa[n % NT] + (t & 1) * 24576 + (n / NT) * 12288;
    f.w[n / NT][n % NT] = trfrag(a, a + 512);
  };
  auto issue = [&](int t, int e) {
    const int v = (t + 5) / 2, half = (t + 1) & 1, j = t + 4;
    if (MODE == 0) {
      if (e < 3) { if (j < 20) issueW1(c, j, e); }
      else if (e == 3) { if (v < 10) issueX(c, v, half); }
    } else if (MODE == 1) {
      if (e < 2 && v < 10) dma16(XS + (v % 3) * 16384 + half * 8192 + (2 * c.w + e) * 1024, c.voff2 ^ (e << 6), c.xbase + (half * 64 + e * 8) * D * 2 + v * 128);
    } else {
      if (e < 6 && j < 20)
        dma16(WS + (j & 3) * 24576 + e * 4096 + (c.w - 4) * 1024, c.voff2, c.wbase + (size_t)j * c.wstep + (e % 3) * 128 + (e / 3) * c.cbd * 2);
    }
  };
  auto step = [&](int t, const Frags& f, Frags& fn) {
    const bool rd = t + 1 < 20;
    if (rd && ((t + 1) & 1) == 0) {
#pragma unroll
      for (int i = 0; i < NT; ++i) { c.wa[i] = ((t + 1) & 2) ? c.wa[i] + 49152 : c.wa[i] - 49152; asm volatile("" : "+v"(c.wa[i])); }
    }
#pragma unroll
    for (int i = 0; i < 8 * NT; ++i) {
      const int n = i >> 2, st = (n & 1) ? 3 - (i & 3) : (i & 3);
      acc[st][n / NT][n % NT] = mfma16(f.w[n / NT][n % NT], f.x[st], acc[st][n / NT][n % NT]);
      if (MODE == 1) { if (i % 8 == 0) issue(t, i >> 3); } else if ((i & 1) == 0) issue(t, i >> 1);
      {
        const int lane = threadIdx.x & 63;
        if (t == 2 && i == 1) {
          asm volatile("" : "+v"(c.epoch), "+v"(c.xrow[0]), "+v"(c.xrow[1]));
          c.tag = __builtin_amdgcn_readfirstlane(c.epoch) + 1u;
          row_sums(c.xrow, c.rsum, c.rsq);
        }
        if (t == 2 && i == 15) {
          const u32x2 g = {row_stat(c.rsum, c.rsq), c.tag};
          __builtin_amdgcn_raw_buffer_store_b64(g, c.rsw, lane == 63 ? W_ROWST + (8 * blockIdx.x + (threadIdx.x >> 6)) * 8 : 0x80000000u, 0, 16);
        }
        if (t == 7 && i == 1) late_setup<NT>(c);
        if (t == 16 && i >= 3 && i < 9) ld_stat(c, i - 3);
        if (t == 18 && i == 0) asm volatile("" : "+v"(c.gr[0]), "+v"(c.gr[1]), "+v"(c.gr[2]), "+v"(c.gr[3]), "+v"(c.gc[0]), "+v"(c.gc[1]));
        if (t >= 18 && (i & 1) && i < 8) {
          const int ks = 8 * (t - 18) + c.w;
          const bool split = ks % 3 == 2;
          w2_stage(c.early ? EAR + c.w * 10240 + (t - 18) * 4096 : SPARE, c.wd + ks * 32 * (D * 2), split ? 4 : 16, split ? 8 : 4, i >> 1, lane);
        }
      }
      if (rd) {
        const int e = NT == 3 ? ((i & 1) ? i >> 1 : -1) : (i < 12 && i % 3 != 2 ? i - i / 3 : -1);
        if (e == 0) readW(fn, t + 1, 0);
        else if (e >= 1 && e <= 4) readX(fn, t + 1, e - 1);
        else if (e >= 5 && e < 4 + 2 * NT) readW(fn, t + 1, e - 4);
      }
      SB();
    }
  };
  Frags f0, f1;
  wait_bar<15>();
  STAMP(1);
#pragma unroll
  for (int i = 0; i < 4; ++i) readX(f0, 0, i);
#pragma unroll
  for (int i = 0; i < 2 * NT; ++i) readW(f0, 0, i);
  constexpr auto cnt = [](int t) {
    return (MODE == 0 ? (t <= 14 ? 4 : t == 15 ? 3 : 0) : MODE == 1 ? (t <= 14 ? 2 : 0) : (t <= 15 ? 6 : 0)) + (t == 2 ? 1 : t == 16 ? 6 : 0);
  };
#define STEP(T, FC, FN) wait_bar<cnt(T - 1) + cnt(T - 2)>(); step(T, FC, FN);
  wait_bar<12>(); step(0, f0, f1);
  wait_bar<7 + cnt(0)>(); step(1, f1, f0);
  STEP(2, f0, f1) STAMP(7); STEP(3, f1, f0) STEP(4, f0, f1)
  STEP(5, f1, f0) STEP(6, f0, f1) STEP(7, f1, f0) STEP(8, f0, f1) STEP(9, f1, f0)
  MSTAMP(8); STEP(10, f0, f1) STEP(11, f1, f0) STEP(12, f0, f1) MSTAMP(10); STEP(13, f1, f0) STEP(14, f0, f1)
  STEP(15, f1, f0) MSTAMP(11); STEP(16, f0, f1) MSTAMP(12);
  STEP(17, f1, f0)
  MSTAMP(13);
  wait_bar<6>();
  MSTAMP(15);
  step(18, f0, f1);
#undef STEP
  wait_vl<63>(); step(19, f1, f0);
}

struct W2 {
  const char *wd, *src7, *src8, *src9;
  int w, so7, gs7, gs8, gs9;
  unsigned ra[2][4];
  DI void init(const u16* wd_, int cgp) {
    w = __builtin_amdgcn_readfirstlane(threadIdx.x >> 6);
    wd = (const char*)wd_ + cgp * 128;
  }
  DI void init_o(const u16* wo_, int cgp) {
    const char* wo = (const char*)wo_ + cgp * 128;
    so7 = w < 4 ? 2 * w : 3 * w - 4;
    auto osrc = [&](int oi) { return wo + (oi >= 16 ? 160 * (oi - 16) + 16 : 40 * oi) * (D * 2); };
    auto ogs = [&](int oi) { return oi >= 16 ? 40 : 4; };
    src7 = osrc(so7); src8 = osrc(so7 + 1); src9 = osrc(so7 + 2);
    gs7 = ogs(so7); gs8 = ogs(so7 + 1); gs9 = ogs(so7 + 2);
    asm volatile("" : "+s"(src7), "+s"(src8), "+s"(src9), "+s"(so7), "+s"(gs7), "+s"(gs8), "+s"(gs9));
  }
  static DI int area(int s) { return (s >> 2) & 1 || s >= 8 ? ((s >> 1) & 1) == 0 : 0; }
  DI unsigned lds(int s) const { return area(s) ? LAT + w * 8192 + (s & 1) * 4096 : EAR + w * 10240 + (s & 1) * 4096; }
  DI int hks(int s) const { return s < 6 ? 8 * s + w : min(48 + w, 53); }
  DI void stage1(int s, int o, int lane, int go) const {
    const bool split = hks(s) % 3 == 2;
    if (s < 7) w2_stage(lds(s), wd + hks(s) * 32 * (D * 2), split ? 4 : 16, split ? 8 : 4, o, lane, go);
    else w2_stage(lds(s), s == 7 ? src7 : s == 8 ? src8 : src9, 20, s == 7 ? gs7 : s == 8 ? gs8 : gs9, o, lane, go);
  }
  DI void stage(int s, int go = -1) const {
    int lane = __builtin_amdgcn_mbcnt_hi(~0u, __builtin_amdgcn_mbcnt_lo(~0u, 0u));
    asm volatile("" : "+v"(lane));
    stage1(s, 0, lane, go); stage1(s, 1, lane, go); stage1(s, 2, lane, go); stage1(s, 3, lane, go);
  }
  DI u32x4 frag(int s, int n) const { const unsigned a = ra[area(s)][n] + (s & 1) * 4096; return trfrag(a, a + 512); }
  DI void frags(u32x4 (&d)[4], int s) const {
#pragma unroll
    for (int n = 0; n < 4; ++n) d[n] = frag(s, n);
  }
  DI void set_ra(int h, int lane) {
    const int G = lane >> 4;
#pragma unroll
    for (int i = 0; i < 4; ++i) {
      ra[h][i] = lds(4 * h) + ((G * 1024 + ((lane >> 2) & 3) * 128 + ((2 * (G & 1) | ((lane >> 3) & 1)) << 5) + (lane & 3) * 8) ^ (((i + w) & 3) << 5));
      asm volatile("" : "+v"(ra[h][i]));
    }
  }
  DI int aslot(int s) const { return s < 7 ? hks(s) : 74 + so7 + s - 7; }
};

template <bool GLU> DI unsigned epilogue1(P1& c, f32x4 (&acc)[4][2][3], W2& W, u32x4 (&wr)[3][4], int cg, char* ws STAMP_ARGS) {
  int tid = threadIdx.x;
  asm volatile("" : "+v"(tid));
  const int lane = tid & 63, w = __builtin_amdgcn_readfirstlane(tid >> 6), sr = lane & 15, G = lane >> 4;
  const int wn = ((w >> 2) << 1) | (w & 1), wm = (w >> 1) & 1, rb = c.rb, lc01 = c.lc01, lc2 = c.lc2;
  constexpr bool glu = GLU;
  const bool isq = cg >= 13, nt3 = glu || w < 4;
  const unsigned tag = c.tag;
  auto bounce = [&]() {
    W.set_ra(0, lane);
    W.frags(wr[0], 0); W.frags(wr[1], 1);
    wait_vl<63>();
    W.stage(2); W.stage(3);
    SB();
  };
  auto stats_ok = [&]() { return __all(c.gr[0].y == tag && c.gr[1].y == tag && c.gr[2].y == tag && c.gr[3].y == tag && c.gc[0].y == tag && c.gc[1].y == tag); };
  if (!stats_ok()) {
#pragma unroll 1
    for (int spin = 0; spin < SPIN_MAX; ++spin) {
#pragma unroll
      for (int e = 0; e < 6; ++e) ld_stat(c, e);
      asm volatile("" : "+v"(c.gr[0]), "+v"(c.gr[1]), "+v"(c.gr[2]), "+v"(c.gr[3]), "+v"(c.gc[0]), "+v"(c.gc[1]));
      if (stats_ok()) break;
      __builtin_amdgcn_s_sleep(2);
    }
  }
  unsigned cs0 = c.gc[0].x, cs1 = c.gc[1].x;
  asm volatile("" : "+v"(cs0), "+v"(cs1));
  float sdv[4], rs[4], rsl[4], ce[4];
#pragma unroll
  for (int st = 0; st < 4; ++st) {
    const unsigned g = c.gr[st].x;
    sdv[st] = hi16(g); rs[st] = __builtin_amdgcn_rcpf(sdv[st]);
    rsl[st] = 1.44269504f * rs[st]; ce[st] = 1e-6f * sdv[st];
    const unsigned mm = (g & 0xffffu) * 0x10001u;
#pragma unroll
    for (int z = 0; z < 6; ++z)
      acc[st][z / 3][z % 3] = mfma16(u32x4{z < 4 ? cs0 : cs1, 0u, 0u, 0u}, u32x4{G == (z & 3) ? mm : 0u, 0u, 0u, 0u}, acc[st][z / 3][z % 3]);
  }
  STAMP(9);
  const rsrc_t rsw = c.rsw;
  const unsigned actb = W_ACT + (8 * rb + 4 * wm) * NSL * 1024 + lane * 16;
  const unsigned acts = W_ACT + (8 * rb + 4 * wm + (G & 1)) * NSL * 1024 + ((2 * (wn & 1) + (G >> 1)) * 16 + sr) * 16;
  auto store_split = [&](int pair, int ks, u32x2 a, u32x2 b) {
    const auto r0 = __builtin_amdgcn_permlane16_swap(a.x, b.x, false, false), r1 = __builtin_amdgcn_permlane16_swap(a.y, b.y, false, false);
    st128(rsw, acts + (2 * pair * NSL + ks) * 1024, u32x4{r0[0], r1[0], r0[1], r1[1]});
  };
  auto phi4 = [&](int st, f32x4 v) {
    return u32x2{pk2(phi(v[0], rs[st], rsl[st], ce[st]), phi(v[1], rs[st], rsl[st], ce[st])), pk2(phi(v[2], rs[st], rsl[st], ce[st]), phi(v[3], rs[st], rsl[st], ce[st]))};
  };
  auto glu4 = [&](int st, int i) {
    float h[4];
#pragma unroll
    for (int j = 0; j < 4; ++j) {
      const float e = __builtin_amdgcn_exp2f(acc[st][0][i][j] * -rsl[st]);
      h[j] = acc[st][1][i][j] * __builtin_amdgcn_rcpf(__builtin_fmaf(e, sdv[st], sdv[st]));
    }
    return u32x2{pk2(h[0], h[1]), pk2(h[2], h[3])};
  };
  if (glu) {
#pragma unroll
    for (int pair = 0; pair < 2; ++pair) {
#pragma unroll
      for (int st = 2 * pair; st < 2 * pair + 2; ++st) {
        const u32x2 a = glu4(st, 0), b = glu4(st, 1);
        st128(rsw, actb + (st * NSL + 6 * cg + (lc01 >> 5)) * 1024, u32x4{a.x, a.y, b.x, b.y});
      }
      store_split(pair, 6 * cg + (lc2 >> 5), glu4(2 * pair, 2), glu4(2 * pair + 1, 2));
    }
  } else if (isq) {
    const int sp0 = 54 + 5 * (cg - 13);
#pragma unroll
    for (int sd = 0; sd < 2; ++sd)
#pragma unroll
      for (int pair = 0; pair < 2; ++pair) {
#pragma unroll
        for (int st = 2 * pair; st < 2 * pair + 2; ++st) {
          const u32x2 a = phi4(st, acc[st][sd][0]), b = phi4(st, acc[st][sd][1]);
          st128(rsw, actb + (st * NSL + sp0 + 10 * sd + (lc01 >> 5)) * 1024, u32x4{a.x, a.y, b.x, b.y});
        }
        if (nt3) store_split(pair, sp0 + 10 * sd + 2, phi4(2 * pair, acc[2 * pair][sd][2]), phi4(2 * pair + 1, acc[2 * pair + 1][sd][2]));
      }
  } else {
    unsigned ia[3][2];
#pragma unroll
    for (int i = 0; i < 3; ++i) {
      const int u = ((i < 2 ? lc01 + 16 * i : lc2) >> 2) + G;
      ia[i][0] = KVI + (64 * wm + sr) * 640 + ((u ^ sr) << 3); ia[i][1] = KVI + (64 * wm + sr) * 640 + (((u + 40) ^ sr) << 3);
    }
#pragma unroll
    for (int i = 0; i < 3; ++i) {
      if (i == 2 && !nt3) break;
#pragma unroll
      for (int st = 0; st < 4; ++st) {
        const f32x4 v = acc[st][1][i];
        *(LAS u32x2*)(ia[i][0] + st * 10240) = phi4(st, acc[st][0][i]);
        *(LAS u32x2*)(ia[i][1] + st * 10240) = u32x2{pk2(v[0] * rs[st], v[1] * rs[st]), pk2(v[2] * rs[st], v[3] * rs[st])};
      }
    }
    KSTAMP(8);
    wait_bar<63>();
    KSTAMP(10);
    const int hf = lane >> 5, q = (lane >> 2) & 3, p = lane & 3;
    const int uk = min(5 * w + 4 * (G & 1) + p, 39);
    unsigned rk[2], rv[2];
#pragma unroll
    for (int t = 0; t < 2; ++t) {
      const int row = 8 * hf + 4 * t + q;
      rk[t] = KVI + row * 640 + ((uk ^ row) << 3); rv[t] = KVI + row * 640 + (((uk + 40) ^ row) << 3);
    }
    u32x4 kf[8], vf[8];
#pragma unroll
    for (int ks = 0; ks < 8; ++ks) {
      const unsigned hi = ks >= 4 ? 40960 : 0;
      kf[ks] = trfrag(rk[0] + hi + (ks & 3) * 10240, rk[1] + hi + (ks & 3) * 10240);
      vf[ks] = trfrag(rv[0] + hi + (ks & 3) * 10240, rv[1] + hi + (ks & 3) * 10240);
    }
    SB();
    f32x16 kv, kz;
#pragma unroll
    for (int i = 0; i < 16; ++i) { kv[i] = 0.f; kz[i] = 0.f; }
    const bool one = (lane & 31) >= 20;
#pragma unroll
    for (int ks = 0; ks < 8; ++ks) {
#pragma unroll
      for (int i = 0; i < 4; ++i) vf[ks][i] = one ? 0x3f803f80u : vf[ks][i];
      if (ks & 1) kz = mfma32(kf[ks], vf[ks], kz); else kv = mfma32(kf[ks], vf[ks], kv);
    }
    kv += kz;
    KSTAMP(11);
#pragma unroll
    for (int j = 0; j < 3; ++j)
      st128(rsw, ((lane & 31) <= 20 && (j < 2 || hf == 0)) ? W_KVP + (rb * 16 + 4 * (cg - 9) + (w >> 1)) * 4096 + ((w & 1) * 105 + (lane & 31) * 5 + 2 * j + hf) * 16 : 0x80000000u,
            __builtin_bit_cast(u32x4, f32x4{kv[4 * j], kv[4 * j + 1], kv[4 * j + 2], kv[4 * j + 3]}));
  }
  STAMP(3);
  if (!glu && !isq) {
    asm volatile("s_waitcnt vmcnt(0)" ::: "memory");
    if (lane < 2) st_flag(ws + W_FLAGKV + ((4 * (cg - 9) + (w >> 1)) * 2 + lane) * 128 + (16 * (w & 1) + rb) * 4, tag);
  } else asm volatile("s_waitcnt vmcnt(0)" ::: "memory");
  if (glu && w < 4) bounce();
  wait_bar<63>();
  if (tid == 0) st_flag(ws + W_FLAG1 + rb * 128 + cg * 4, tag);
  if (glu && w >= 4) bounce();
  return tag;
}

template <bool GLU> DI unsigned phase1(int rb, int cg, int cgp, W2& W, u32x4 (&wr)[3][4], const u16* x, const u16* wqkv, const u16* wg, const u16* wu, const u16* wd, char* ws STAMP_ARGS) {
  const int tid = threadIdx.x, lane = tid & 63, w = __builtin_amdgcn_readfirstlane(tid >> 6);
  const int wn = ((w >> 2) << 1) | (w & 1), wm = (w >> 1) & 1;
  P1 c;
  const int side = w & 1, oct = w >> 1, rho = lane >> 3, pi = lane & 7;
  c.voffx = (8 * w + rho) * (D * 2) + ((pi ^ (4 * (w & 1) + (rho >> 1))) << 4);
  c.xbase = (const char*)x + (size_t)rb * 128 * D * 2;
  c.wdst = WS + (side * 12 + oct) * 1024; c.xdst = XS + w * 1024;
  issueX(c, 0, 0); issueX(c, 0, 1);
  constexpr bool glu = GLU;
  const bool isq = cg >= 13;
  auto col0 = [&](int sd) { return glu ? 192 * cg : isq ? 160 * (cg - 13) + 320 * sd : 640 + 640 * sd + 160 * (cg - 9); };
  const int shift = (!glu && !(cg & 1)) ? 32 : 0;
  c.wbase = glu ? (side ? (const char*)wu : (const char*)wg) : (const char*)wqkv;
  const int pitch = glu ? F * 2 : 3 * D * 2;
  c.voff = (col0(side) - shift + 16 * ((pi >> 1) ^ (2 * (oct & 1) | ((rho >> 1) & 1)))) * 2 + (8 * oct + rho) * pitch + (pi & 1) * 16;
  c.wstep = 32 * pitch;
  issueW1(c, 0, 0); issueW1(c, 0, 1); issueW1(c, 0, 2);
  STAMP(5);
  SB(); __builtin_amdgcn_s_barrier(); SB();
  issueW1(c, 1, 0); issueW1(c, 1, 1); issueW1(c, 1, 2); issueX(c, 1, 0);
  SB();
  const int sr = lane & 15, G = lane >> 4;
  const int lc01 = 32 * (3 * (wn >> 1) + (wn & 1)), lc2 = 32 * (3 * (wn >> 1) + 2) + 16 * (wn & 1);
  c.xa[0] = XS + (64 * wm + sr) * 128 + ((G ^ (sr >> 1)) << 4); c.xa[1] = c.xa[0] ^ 64;
  {
    const int q = (lane >> 2) & 3, p = lane & 3;
    const unsigned b0 = WS + G * 1024 + q * 128 + p * 8, sw = 2 * (G & 1) | (q >> 1);
#pragma unroll
    for (int i = 0; i < 3; ++i) { const int lc = (i < 2 ? lc01 + 16 * i : lc2) + shift; c.wa[i] = b0 + (lc >> 6) * 4096 + ((((lc >> 4) & 3) ^ sw) << 5); }
  }
  asm volatile("" : "+v"(c.xa[0]), "+v"(c.xa[1]), "+v"(c.wa[0]), "+v"(c.wa[1]), "+v"(c.wa[2]));
  SB();
  issueW1(c, 2, 0); issueW1(c, 2, 1); issueW1(c, 2, 2); issueX(c, 1, 1);
  issueW1(c, 3, 0); issueW1(c, 3, 1); issueW1(c, 3, 2); issueX(c, 2, 0);
  {
    const char* xr = (const char*)x + (size_t)(8 * blockIdx.x + w) * (D * 2);
    gload<0>(c.xrow[0], min(lane, 39) * 32, xr); gload<16>(c.xrow[1], min(lane, 39) * 32, xr);
    gload32(c.epoch, W_EPOCH, ws);
  }
  SB();
  c.rsw = mk_rsrc(ws);
  c.ws = ws; c.early = glu || isq; c.rb = rb; c.lc01 = lc01; c.lc2 = lc2; c.w = w;
  c.cb0 = col0(0) + (glu ? 0 : 2 * F); c.cbd = glu ? F : isq ? 320 : 640;
  c.wd = (const char*)wd + cgp * 128;
  f32x4 acc[4][2][3];
#pragma unroll
  for (int i = 0; i < 24; ++i) acc[i / 6][(i / 3) & 1][i % 3] = f32x4{0.f, 0.f, 0.f, 0.f};
  if (GLU) mainloop<0>(c, acc STAMP_PASS);
  else if (w < 4) {
    c.voff2 = (16 * w + rho) * (D * 2) + ((pi ^ (rho >> 1)) << 4);
    mainloop<1>(c, acc STAMP_PASS);
  } else {
    c.voff2 = (col0(0) - shift + 16 * ((pi >> 1) ^ (2 * (w & 1) | ((rho >> 1) & 1)))) * 2 + (8 * (w - 4) + rho) * pitch + (pi & 1) * 16;
    mainloop<2>(c, acc STAMP_PASS);
  }
  STAMP(2);
  return epilogue1<GLU>(c, acc, W, wr, cg, ws STAMP_PASS);
}

template <class F1> DI void o_share(int P, int rbo, int nrb, unsigned fo, unsigned tag, char* ws, int lane, F1 w1 STAMP_ARGS) {
  const int w = __builtin_amdgcn_readfirstlane(threadIdx.x >> 6), sr = lane & 15, G = lane >> 4;
  const rsrc_t rsw = mk_rsrc(ws);
  const unsigned l16 = lane * 16;
  if (w == 0) poll(lane < 32 ? ws + W_FLAGKV + (P * 2 + ((rbo >> 1) & 1)) * 128 + lane * 4 : ws + W_FLAG1 + (rbo + lane - 32) * 128 + (13 + ((P >> 2) & 1)) * 4,
                   lane < 32 + nrb, tag);
  else w1();
  const int s0 = (40 * P) / 32;
  unsigned ta[8][2];
#pragma unroll
  for (int i = 0; i < 8; ++i) {
    const int b = i >> 2, sp = s0 + ((i >> 1) & 1);
    const int e = (i & 1) == 0 ? sr : (sr & 3) == b ? 20 : ((sr & 2) && (sr >> 3) == b) ? 16 + 2 * ((sr >> 2) & 1) + (sr & 1) : -1;
#pragma unroll
    for (int hl = 0; hl < 2; ++hl) {
      const int d = 32 * sp + (sp % 5 == 2 ? 8 * G + 4 * hl : 16 * hl + 4 * G) - 20 * (2 * P + b);
      ta[i][hl] = (e >= 0 && d >= 0 && d < 20) ? T2 + b * 840 + e * 40 + d * 2 : T2 + 1680;
      asm volatile("" : "+v"(ta[i][hl]));
    }
  }
  if (w == 7) *(LAS u32x2*)(T2 + 1680) = u32x2{0u, 0u};
  wait_bar<63>();
  XSTAMP(10);
  const unsigned tile = W_ACT + (8 * rbo + w) * NSL * 1024;
  u32x4 pqf[3][2], part[16];
  const bool pact = w >= 4 && (w - 4) * 64 + lane < 210;
  if (pact) {
#pragma unroll
    for (int i = 0; i < 16; ++i) part[i] = ld128s(rsw, l16, W_KVP + (i * 16 + P) * 4096 + (w - 4) * 1024);
  }
#pragma unroll
  for (int r = 0; r < 3; ++r)
#pragma unroll
    for (int i = 0; i < 2; ++i) pqf[r][i] = ld128s(rsw, r < 2 || nrb == 3 ? l16 : 0x80000000u, tile + (r * 8 * NSL + 54 + s0 + i) * 1024);
  const unsigned hf = ld32(rsw, W_FLAG1 + fo);
  SB();
  if (pact) {
    f32x4 t[4];
#pragma unroll
    for (int i = 0; i < 4; ++i)
      t[i] = (__builtin_bit_cast(f32x4, part[4 * i]) + __builtin_bit_cast(f32x4, part[4 * i + 1])) + (__builtin_bit_cast(f32x4, part[4 * i + 2]) + __builtin_bit_cast(f32x4, part[4 * i + 3]));
    t[0] = (t[0] + t[1]) + (t[2] + t[3]);
    *(LAS u32x2*)(T2 + ((w - 4) * 64 + lane) * 8) = u32x2{pk2(t[0][0], t[0][1]), pk2(t[0][2], t[0][3])};
  }
  OSTAMP(2);
  wait_bar<63>();
  OSTAMP(3);
  u32x4 a[8];
#pragma unroll
  for (int i = 0; i < 8; ++i) { const u32x2 lo = *(LAS u32x2*)ta[i][0], hi = *(LAS u32x2*)ta[i][1]; a[i] = u32x4{lo.x, lo.y, hi.x, hi.y}; }
#pragma unroll
  for (int r = 0; r < 3; ++r) {
    const f32x4 z = {0.f, 0.f, 0.f, 0.f};
    const f32x4 na = mfma16(a[2], pqf[r][1], mfma16(a[0], pqf[r][0], z)), nb = mfma16(a[6], pqf[r][1], mfma16(a[4], pqf[r][0], z));
    const f32x4 xx = mfma16(a[7], pqf[r][1], mfma16(a[5], pqf[r][0], mfma16(a[3], pqf[r][1], mfma16(a[1], pqf[r][0], z))));
    const float ra = __builtin_amdgcn_rcpf(xx[0] + 1e-6f), rb = __builtin_amdgcn_rcpf(xx[1] + 1e-6f), rl = G < 2 ? ra : rb;
    const unsigned tr = (r < 2 || nrb == 3) ? tile + r * 8 * NSL * 1024 : 0x80000000u;
    st128(rsw, tr + (74 + P) * 1024 + l16, u32x4{pk2(na[0] * ra, na[1] * ra), pk2(na[2] * ra, na[3] * ra), pk2(nb[0] * rb, nb[1] * rb), pk2(nb[2] * rb, nb[3] * rb)});
    st32(rsw, tr + (90 + (P >> 2)) * 1024 + ((P & 3) * 16 + sr) * 16 + 4 * G, pk2(xx[2] * rl, xx[3] * rl));
  }
  OSTAMP(5);
  asm volatile("s_waitcnt vmcnt(0)" ::: "memory");
  if (lane < nrb) st_flag(ws + W_FLAGO + ((8 * (rbo + lane) + w) * 16 + P) * 4, tag);
  SB();
  if (w == 0) w1();
  if (!__all(sr >= 9 || hf == tag)) poll(ws + W_FLAG1 + fo, sr < 9, tag);
}

template <int NS>
DI void tail(W2& W, u32x4 (&wr)[3][4], int st0, int cgp, int P, int jj, int pre, unsigned tag, const u16* x, const u16* wo, u16* out, char* ws STAMP_ARGS) {
  int lane = __builtin_amdgcn_mbcnt_hi(~0u, __builtin_amdgcn_mbcnt_lo(~0u, 0u));
  asm volatile("" : "+v"(lane));
  const int w = W.w, sr = lane & 15;
  constexpr int RW = NS == 6 ? 3 : 2;
  W.init_o(wo, cgp);
  W.set_ra(0, lane); W.set_ra(1, lane);
  const rsrc_t rsw = mk_rsrc(ws);
  const unsigned l16 = lane * 16;
  const unsigned fo = ((lane & 16) ? (st0 + NS - 1) >> 3 : st0 >> 3) * 128 + sr * 4;
  const unsigned mail = 2032 + (w == 0 ? (lane & 7) : w) * 2048;
  if (w == 0) *(LAS unsigned*)mail = ~tag;
  const int rot = w >> 2;
  auto gst = [&](int st) { return st == NS - 1 ? (rot ? 0 : NS - 1) : st + rot; };
  const unsigned abase = W_ACT + st0 * NSL * 1024;
  constexpr int AH = NS == 6 ? 2 : 5;
  u32x4 a[10][NS], wl[4];
  auto issueA1 = [&](int s, int st) { a[s][st] = ld128s(rsw, l16, abase + (W.aslot(s) + gst(st) * NSL) * 1024); };
  auto first = [&]() {
#pragma unroll
    for (int e = 0; e < AH * NS; ++e) issueA1(e / NS, e % NS);
    SB();
  };
  if (NS == 6) {
    W.stage(4); W.stage(5);
    wait_v<8>();
    W.frags(wr[2], 2);
    wait_vl<63>();
    W.stage(6);
    XSTAMP(8);
    if (w == 0) poll(ws + W_FLAG1 + fo, sr < 9, tag);
    wait_bar<63>();
  } else {
    o_share(P, jj < 2 ? 3 * jj : 2 + 2 * jj, jj < 2 ? 3 : 2, fo, tag, ws, lane, [&]() {
      if (pre == 0) { W.stage(0); W.stage(1); }
      W.stage(4); W.stage(5);
      SB();
    } STAMP_PASS);
    XSTAMP(8);
    wait_v<8>();
    W.frags(wr[0], 0); W.frags(wr[1], 1);
    wait_vl<63>();
    W.stage(2); W.stage(3);
  }
  first();
  XSTAMP(11);
  __builtin_amdgcn_s_setprio(3);
  f32x4 acc[NS][4];
  u32x2 oflag;
  const unsigned oo = W_FLAGO + st0 * 64 + min(lane, 8 * NS - 1) * 8;
  auto vm = [&](int s, int e) {
    if (RW == 2 && s == 3 && e == 0) W.stage(6);
    if (s == 4 && e == 0) W.stage(7);
    if (s + AH <= 6 && e < NS) issueA1(s + AH, e);
    if (s == 4 && e == NS) oflag = ld64(rsw, oo);
    if (s == 5 && e == 0) W.stage(8);
  };
  auto pslot = [&](int src, int l) { const int k = l - 1 - (NS > 3 && l > 8) - (NS == 6 && l > 16); return k < 10 ? EAR + src * 10240 + k * 1024 : k < 18 ? LAT + src * 8192 + (k - 10) * 1024 : src * 2048 + (k - 18) * 1024; };
  auto put = [&](int j) {
    const int pn = j / NS, pst = (pn & 1) ? NS - 1 - j % NS : j % NS, l = 4 * pst + pn;
    if (l != 0 && (l != 8 || NS == 3) && (l != 16 || NS != 6) && l < 22) *(LAS f32x4*)(pslot(w, l) + l16) = acc[pst][pn];
  };
  auto slot = [&](int s, const u32x4 (&wc)[4], bool last) {
    if (s == 2) __builtin_amdgcn_s_setprio(2);
    if (s == 4) __builtin_amdgcn_s_setprio(1);
    if (s == 7) __builtin_amdgcn_s_setprio(0);
#pragma unroll
    for (int i = 0; i < 4 * NS; ++i) {
      const int n = i / NS, st = (n & 1) ? NS - 1 - i % NS : i % NS;
      if (s == 0) mfma16_new(acc[st][n], wc[n], a[s][st]); else mfma16_acc(acc[st][n], wc[n], a[s][st]);
      if ((i & 1) == 0) vm(s, i >> 1);
      if (s >= RW - 1 && s <= 7 && s != 6 && i % NS == NS - 1) wl[n] = W.frag(s + 1, n);
      if (last && i >= 8) put(i - 8);
      SB();
    }
    if (last) {
      asm volatile("s_nop 7");
      SB();
#pragma unroll
      for (int j = 4 * NS - 8; j < 4 * NS; ++j) put(j);
    }
  };
  P2STAMP(0); slot(0, wr[0], false);
  P2STAMP(1); slot(1, wr[1], false);
  P2STAMP(2); slot(2, RW == 3 ? wr[2] : wl, false);
  P2STAMP(3); slot(3, wl, false);
  P2STAMP(4); slot(4, wl, false);
  if (RW == 2) wait_v<5>();
  slot(5, wl, false);
  if (!__all(oflag.x == tag && oflag.y == tag)) {
#pragma unroll 1
    for (int spin = 0; spin < SPIN_MAX; ++spin) {
      u32x2 f;
      if (w == 0) f = ld64(rsw, oo); else f.x = f.y = *(volatile LAS unsigned*)mail;
      if (__all(f.x == tag && f.y == tag)) break;
      __builtin_amdgcn_s_sleep(POLL_SLEEP);
    }
    __builtin_amdgcn_fence(__ATOMIC_ACQUIRE, "wavefront");
  }
  if (w == 0) *(volatile LAS unsigned*)mail = tag;
  SB();
  XSTAMP(15);
#pragma unroll
  for (int e = 0; e < 2 * NS; ++e) issueA1(7 + e / NS, e % NS);
  int tz = __builtin_amdgcn_mbcnt_hi(~0u, __builtin_amdgcn_mbcnt_lo(~0u, 0u));
  asm volatile("" : "+v"(tz));
  const unsigned eo = (16 * (st0 + rot) + (tz & 15)) * D + 64 * cgp + 16 * (w & 3) + 4 * (tz >> 4);
  constexpr int NI = (NS + 1) / 2;
  u32x2 xres[NI];
#pragma unroll
  for (int i = 0; i < NI; ++i) xres[i] = *(const GAS u32x2*)(x + (i == NI - 1 && (NS & 1) && rot ? 0 : 32 * i * D) + eo);
  SB();
  u32x4 wx[4];
  W.frags(wx, 7);
  SB();
  if (w < 6) slot(6, wl, false);
  if (rot) {
    W.stage(9);
#pragma unroll
    for (int e = 0; e < NS; ++e) issueA1(9, e);
  }
  XSTAMP(12);
  slot(7, wx, false);
  if (rot) {
    wait_v<0>();
    W.frags(wx, 9);
    slot(9, wx, false);
  }
  slot(8, wl, true);
  XSTAMP(13);
  wait_bar<63>();
  OSTAMP(7);
  auto finish = [&](int i, f32x4 s) {
    const u32x2 o = {pk2(s[0] + lo16(xres[i].x), s[1] + hi16(xres[i].x)), pk2(s[2] + lo16(xres[i].y), s[3] + hi16(xres[i].y))};
    __hip_atomic_store((GAS u64*)(out + 32 * i * D + eo), __builtin_bit_cast(u64, o), RLX_AGENT);
  };
  f32x4 v[NI][8];
#pragma unroll
  for (int j = 1; j < 8; ++j) {
    const int dh = j >> 2, dn = j & 3, sw = 4 * (rot ^ dh) + ((w - dn) & 3);
#pragma unroll
    for (int i = 0; i < NI; ++i) {
      const int l0 = 4 * (dh ? (2 * i + NS - 1) % NS : 2 * i) + dn, l1 = 4 * (dh ? (2 * i + 1) % NS : 2 * i) + dn;
      const int a0 = pslot(sw, min(l0, 21)), a1 = pslot(sw, min(l1, 21));
      v[i][j] = *(LAS f32x4*)((rot ? a1 : a0) + l16);
      if (l0 >= 22 || l1 >= 22) v[i][j] *= (rot ? l1 : l0) >= 22 ? 0.f : 1.f;
    }
  }
  f32x4 sum[NI];
#pragma unroll
  for (int i = 0; i < NI; ++i) {
    sum[i] = ((acc[2 * i][0] + v[i][1]) + (v[i][2] + v[i][3])) + ((v[i][4] + v[i][5]) + (v[i][6] + v[i][7]));
    if (NS == 6 ? i == 1 : (i < NI - 1 || !(NS & 1) || !rot)) finish(i, sum[i]);
  }
  if constexpr (NS == 6) {
    wait_bar<63>();
    *(LAS f32x4*)((w * 2) * 1024 + l16) = acc[5][2];
    *(LAS f32x4*)((w * 2 + 1) * 1024 + l16) = acc[5][3];
    wait_bar<63>();
    const int o4 = rot ? 0 : 4;
    const f32x4 e = *(LAS f32x4*)((((o4 + ((w - 2) & 3)) * 2)) * 1024 + l16) + *(LAS f32x4*)(((o4 + ((w - 3) & 3)) * 2 + 1) * 1024 + l16);
    if (rot) sum[2] += e; else sum[0] += e;
    finish(0, sum[0]); finish(2, sum[2]);
  }
}

__global__ __launch_bounds__(512, 2) void fused(const u16* __restrict__ x, const u16* __restrict__ wqkv, const u16* __restrict__ wg,
                                                const u16* __restrict__ wu, const u16* __restrict__ wd, const u16* __restrict__ wo,
                                                u16* __restrict__ out, char* __restrict__ ws) {
  asm volatile("" : "+s"(x), "+s"(wqkv), "+s"(wg), "+s"(wu), "+s"(wd), "+s"(wo), "+s"(out), "+s"(ws));
  const int xcd = blockIdx.x & 7, idx = blockIdx.x >> 3;
  STAMP_DECL
  STAMP(0);
  unsigned tag;
  const int half = xcd & 1;
  const int i7 = idx / 7, j7 = idx - 7 * i7;
  const int cg = idx >= 28 ? 4 : j7 < 4 ? 5 * half + j7 : j7 < 6 ? 5 + 2 * half + j7 : 13 + half;
  const int rb = 4 * (xcd >> 1) + (idx >= 28 ? 2 * half + idx - 28 : i7);
  const int lr = idx >= 30 ? idx : idx >= 28 ? idx - 12 : j7 < 4 ? 4 * i7 + j7 : j7 < 6 ? 14 + 2 * i7 + j7 : 26 + i7;
  const u64 tb = lr < 8 ? TB0 : lr < 16 ? TB1 : lr < 24 ? TB2 : TB3;
  const int te = (int)(tb >> (8 * (lr & 7))) & 255;
  const int st0 = 32 * (xcd >> 1) + (te & 31), cgp = 5 * half + (te >> 5);
  if (lr < 18) {
    W2 W;
    u32x4 wr[3][4];
    W.init(wd, cgp);
    tag = phase1<true>(rb, cg, cgp, W, wr, x, wqkv, wg, wu, wd, ws STAMP_PASS);
    STAMP(4);
    tail<6>(W, wr, st0, cgp, 0, 0, 2, tag, x, wo, out, ws STAMP_PASS);
  } else {
    W2 W;
    u32x4 wr[3][4];
    W.init(wd, cgp);
    if (idx >= 30) {
      tag = helper(half, 2 * (xcd >> 1) + idx - 30, x, wqkv, wg, wu, wd, wo, ws STAMP_PASS);
      wait_bar<63>();
    } else tag = phase1<false>(rb, cg, cgp, W, wr, x, wqkv, wg, wu, wd, ws STAMP_PASS);
    STAMP(4);
    const int P = 2 * xcd + (lr & 1), jj = lr >= 30 ? 0 : (lr - 16) >> 1;
    if (lr < 26 || lr >= 30) tail<4>(W, wr, st0, cgp, P, jj, 0, tag, x, wo, out, ws STAMP_PASS);
    else tail<3>(W, wr, st0, cgp, P, jj, 1, tag, x, wo, out, ws STAMP_PASS);
  }
  if (blockIdx.x == 255 && threadIdx.x < 64) {
    const int lane = threadIdx.x & 63;
#pragma unroll
    for (int i = 0; i < 4; ++i) {
      poll(ws + W_FLAG1 + (4 * i + (lane >> 4)) * 128 + (lane & 15) * 4, (lane & 15) < 15, tag);
      poll(ws + W_FLAGO + (4 * i + (lane >> 4)) * 512 + (lane & 15) * 4, true, tag);
    }
    if (lane == 0) st_flag(ws + W_EPOCH, tag);
  }
  STAMP(14);
  if (blockIdx.x == PBLOCK && threadIdx.x < 64) {
    const unsigned t0 = __builtin_amdgcn_readlane(st_t, 0), c0 = __builtin_amdgcn_readlane(st_c, 0);
    const unsigned v = PCYC ? (__builtin_amdgcn_readlane(st_c, PROBE) - c0) / 20u : __builtin_amdgcn_readlane(st_t, PROBE) - t0;
    for (int i = 0; i < 4000000; ++i) { if ((unsigned)__builtin_amdgcn_s_memrealtime() - t0 >= 10000u + v) break; __builtin_amdgcn_s_sleep(1); }
  }
}

__global__ void tiny(char* ws) { if (threadIdx.x == 0) *(volatile unsigned*)(ws + (64 << 20)) = 1u; }
__global__ void spin100(char* ws) { const unsigned t0 = (unsigned)__builtin_amdgcn_s_memrealtime(); for (int i = 0; i < 4000000; ++i) { if ((unsigned)__builtin_amdgcn_s_memrealtime() - t0 >= 10000u) break; __builtin_amdgcn_s_sleep(1); } if (threadIdx.x == 0) *(volatile unsigned*)(ws + (64 << 20)) = 1u; }
extern "C" void kernel_launch(void* const* d_in, const int* in_sizes, int n_in, void* d_out, int out_size, void* d_ws, size_t ws_size,
                              hipStream_t stream) {
  const u16* x = (const u16*)d_in[0]; const u16* wqkv = (const u16*)d_in[1]; const u16* wo = (const u16*)d_in[2];
  const u16* wg = (const u16*)d_in[4]; const u16* wu = (const u16*)d_in[5]; const u16* wd = (const u16*)d_in[6];
  (void)hipFuncSetAttribute((const void*)fused, hipFuncAttributeMaxDynamicSharedMemorySize, LDS_BYTES);
  fused<<<256, 512, LDS_BYTES, stream>>>(x, wqkv, wg, wu, wd, wo, (u16*)d_out, (char*)d_ws);
  tiny<<<1, 64, 0, stream>>>((char*)d_ws);
}
```

```cpp
#include <hip/hip_runtime.h>
#include <hip/hip_bf16.h>
#define PF_EARLY 16
#define PF_LOOP 10
#define SPREAD 2
#define PRIO 1
#define WARM 1
#define LEAN 0
#define KROT 1
#define HALF 0
#define HT 1
#define PRE 30
#define PV 0
#define K16 1
#define P1PRIO 0
#define LATEST 0
#define GDELAY 0
#define OUTSC1 0
#define EB 1
#define QE 0
#define QP 1
#define CWARM 1
#define CWARM0 1
#define JOBABL 0
#define ACTMODE 0
#define POLL_SLEEP 4
typedef unsigned short u16;
typedef unsigned long long u64;
typedef short s16x4 __attribute__((ext_vector_type(4)));
typedef __bf16 bf16x2 __attribute__((ext_vector_type(2)));
typedef __bf16 bf16x8 __attribute__((ext_vector_type(8)));
typedef float f32x16 __attribute__((ext_vector_type(16)));
typedef float f32x4 __attribute__((ext_vector_type(4)));
typedef unsigned u32x4 __attribute__((ext_vector_type(4)));
typedef unsigned u32x2 __attribute__((ext_vector_type(2)));
#define LAS __attribute__((address_space(3)))
#define GAS __attribute__((address_space(1)))
#define DI __device__ __forceinline__
#define RLX_AGENT __ATOMIC_RELAXED, __HIP_MEMORY_SCOPE_AGENT

constexpr int S = 2048, D = 640, F = 1728;
constexpr int NSL = 94;
constexpr int XS = 0, WS = 49152, LDS_BYTES = 163840;
constexpr int KVI = 16384;
constexpr int SPIN_MAX = 1 << 14;
constexpr unsigned W_EPOCH = 0, W_FLAG1 = 96 << 10  , W_FLAGKV = 128 << 10  ,
                   W_FLAGO = 256 << 10  ,
                   W_ROWST = 16384, W_COLST = W_ROWST + 2048 * 8, W_ACT = 1 << 20  ,
                   W_KVP = 14 << 20  , W_STAMP = 20 << 20;
constexpr int CODE_KB = 96;
constexpr int FL = 128;
constexpr int SPARE = 147456;
constexpr int EAR = 16384, LAT = 98304;
constexpr int T2 = 0;

DI float rbf(float f) { return (float)(__bf16)f; }
DI unsigned pk2(float a, float b) { bf16x2 v = {(__bf16)a, (__bf16)b}; return __builtin_bit_cast(unsigned, v); }
DI float lo16(unsigned u) { return __uint_as_float(u << 16); }
DI float hi16(unsigned u) { return __uint_as_float(u & 0xffff0000u); }
DI float dot2(unsigned a, unsigned b, float c) { return __builtin_amdgcn_fdot2_f32_bf16(__builtin_bit_cast(bf16x2, a), __builtin_bit_cast(bf16x2, b), c, false); }
DI f32x16 mfma32(u32x4 a, u32x4 b, f32x16 c) { return __builtin_amdgcn_mfma_f32_32x32x16_bf16(__builtin_bit_cast(bf16x8, a), __builtin_bit_cast(bf16x8, b), c, 0, 0, 0); }
DI f32x4 mfma16(u32x4 a, u32x4 b, f32x4 c) { return __builtin_amdgcn_mfma_f32_16x16x32_bf16(__builtin_bit_cast(bf16x8, a), __builtin_bit_cast(bf16x8, b), c, 0, 0, 0); }
DI void mfma16_acc(f32x4& c, u32x4 a, u32x4 b) { asm volatile("v_mfma_f32_16x16x32_bf16 %0, %1, %2, %0" : "+v"(c) : "v"(a), "v"(b)); }
DI void mfma16_new(f32x4& c, u32x4 a, u32x4 b) { asm volatile("v_mfma_f32_16x16x32_bf16 %0, %1, %2, 0" : "=&v"(c) : "v"(a), "v"(b)); }
DI f32x4 mfma16k16(unsigned a, unsigned b, f32x4 c) {
  return __builtin_amdgcn_mfma_f32_16x16x16bf16_1k(__builtin_bit_cast(s16x4, u32x2{a, 0u}), __builtin_bit_cast(s16x4, u32x2{b, 0u}), c, 0, 0, 0);
}
DI void dma16(unsigned ldsaddr, unsigned voff, const char* sbase) {
  asm volatile("s_mov_b32 m0, %0\n\ts_nop 0\n\tglobal_load_lds_dwordx4 %1, %2" ::"s"(ldsaddr), "v"(voff), "s"(sbase));
}
DI void dma16p(int go, unsigned ldsaddr, unsigned voff, const char* sbase) {
  asm volatile("s_cmp_eq_u32 %3, 0\n\ts_cbranch_scc1 .Lskip%=\n\ts_mov_b32 m0, %0\n\ts_nop 0\n\tglobal_load_lds_dwordx4 %1, %2\n.Lskip%=:"
               ::"s"(ldsaddr), "v"(voff), "s"(sbase), "s"(go) : "scc");
}
DI void aload(u32x4& d, unsigned voff, const char* sbase) { asm volatile("global_load_dwordx4 %0, %1, %2 sc1" : "=v"(d) : "v"(voff), "s"(sbase)); }
DI void gload32(unsigned& d, unsigned voff, const char* sbase) { asm volatile("global_load_dword %0, %1, %2" : "=v"(d) : "v"(voff), "s"(sbase)); }
template <int OFF> DI void gload(u32x4& d, unsigned voff, const char* sbase) { asm volatile("global_load_dwordx4 %0, %1, %2 offset:%3" : "=v"(d) : "v"(voff), "s"(sbase), "i"(OFF)); }
DI void aload32(unsigned& d, unsigned voff, const char* sbase) { asm volatile("global_load_dword %0, %1, %2 sc1" : "=v"(d) : "v"(voff), "s"(sbase)); }
DI void sload_epoch(unsigned& e, const char* ws) { asm volatile("s_load_dword %0, %1, 0x0\n\ts_waitcnt lgkmcnt(0)" : "=s"(e) : "s"(ws)); }
DI u32x2 trread(unsigned addr) { return __builtin_bit_cast(u32x2, __builtin_amdgcn_ds_read_tr16_b64_v4i16((LAS s16x4*)addr)); }
DI u32x4 trfrag(unsigned a0, unsigned a1) { const u32x2 lo = trread(a0), hi = trread(a1); return u32x4{lo.x, lo.y, hi.x, hi.y}; }
DI u32x4 ldsread128(unsigned addr) { return *(LAS u32x4*)addr; }
DI void st_gran(char* p, unsigned tag, unsigned v) { __hip_atomic_store((GAS u64*)p, ((u64)tag << 32) | v, RLX_AGENT); }
DI unsigned ld_flag(const char* p) { return __hip_atomic_load((const GAS unsigned*)p, RLX_AGENT); }
DI void st_flag(char* p, unsigned v) { __hip_atomic_store((GAS unsigned*)p, v, RLX_AGENT); }
typedef __amdgpu_buffer_rsrc_t rsrc_t;
DI rsrc_t mk_rsrc(char* ws) { return __builtin_amdgcn_make_buffer_rsrc(ws, 0, 0x7fffffff, 0x00020000); }
DI void st128(rsrc_t rs, unsigned off, u32x4 v) { __builtin_amdgcn_raw_buffer_store_b128(v, rs, off, 0, 16); }
DI void st32(rsrc_t rs, unsigned off, unsigned v) { __builtin_amdgcn_raw_buffer_store_b32(v, rs, off, 0, 16); }
DI u32x4 ld128(rsrc_t rs, unsigned off) { return __builtin_amdgcn_raw_buffer_load_b128(rs, off, 0, 16); }
DI u32x4 ld128s(rsrc_t rs, unsigned voff, unsigned soff) { return __builtin_amdgcn_raw_buffer_load_b128(rs, voff, soff, 16); }
DI u32x2 ld64(rsrc_t rs, unsigned off) { return __builtin_amdgcn_raw_buffer_load_b64(rs, off, 0, 16); }
DI unsigned ld32(rsrc_t rs, unsigned off) { return __builtin_amdgcn_raw_buffer_load_b32(rs, off, 0, 16); }
DI void st_flags(rsrc_t rs, unsigned off, int n, int lane, unsigned tag) { st128(rs, (lane >> 3) < n ? off + (lane & 7) * 16 : 0x80000000u, u32x4{tag, tag, tag, tag}); }
DI float exp2_sat(float t) { return __builtin_amdgcn_fmed3f(__builtin_amdgcn_exp2f(t), 0.f, 1.f); }
DI float vmax(float a, float b) { float r; asm("v_max_f32 %0, %1, %2" : "=v"(r) : "v"(a), "v"(b)); return r; }
DI float phi(float a, float rs, float rsl, float ce) { return __builtin_fmaf(vmax(a, ce), rs, exp2_sat(a * rsl)); }

#define SB() __builtin_amdgcn_sched_barrier(0)
#define FENCE() asm volatile("" ::: "memory")
#define WCNT(V, L) (((V) & 15) | (7 << 4) | ((L) << 8) | (((V) >> 4) << 14))
template <int N> DI void wait_v() { SB(); FENCE(); __builtin_amdgcn_s_waitcnt(WCNT(N, 15)); FENCE(); SB(); }
template <int N> DI void wait_vl() { SB(); FENCE(); __builtin_amdgcn_s_waitcnt(WCNT(N, 0)); FENCE(); SB(); }
template <int N> DI void wait_bar() { SB(); FENCE(); __builtin_amdgcn_s_waitcnt(WCNT(N, 0)); __builtin_amdgcn_s_barrier(); FENCE(); SB(); }
DI void drain_bar() { SB(); asm volatile("s_waitcnt vmcnt(0) lgkmcnt(0)\n\ts_barrier" ::: "memory"); SB(); }
DI void poll(const char* p, bool act, unsigned tag) {
#pragma unroll 1
  for (int spin = 0; spin < SPIN_MAX; ++spin) {
    if (__all(!act || ld_flag(p) == tag)) break;
    __builtin_amdgcn_s_sleep(POLL_SLEEP);
  }
  __builtin_amdgcn_fence(__ATOMIC_ACQUIRE, "wavefront");
}
#define STAMP(i)
#define STAMP_DECL
#define STAMP_OUT
#define STAMP_ARGS
#define STAMP_PASS

template <int CTRL, int RM> DI float dpp_add(float v) {
  return v + __builtin_bit_cast(float, __builtin_amdgcn_update_dpp(0, __builtin_bit_cast(int, v), CTRL, RM, 0xf, true));
}
DI float wave_sum(float v) {
  v = dpp_add<0x111, 0xf>(v); v = dpp_add<0x112, 0xf>(v); v = dpp_add<0x114, 0xf>(v); v = dpp_add<0x118, 0xf>(v);
  v = dpp_add<0x142, 0xa>(v); return dpp_add<0x143, 0xc>(v);
}
DI void row_sums(const u32x4 (&xr)[2], float& s, float& q) {
  s = 0.f; q = 0.f;
#pragma unroll
  for (int i = 0; i < 8; ++i) { s = dot2(xr[i >> 2][i & 3], 0x3f803f80u, s); q = dot2(xr[i >> 2][i & 3], xr[i >> 2][i & 3], q); }
  const bool on = (threadIdx.x & 63) < 40;
  s = wave_sum(on ? s : 0.f); q = wave_sum(on ? q : 0.f);
}
DI unsigned row_stat(float s, float q) {
  const float m = s * (1.f / D);
  return pk2(-m, sqrtf(rbf(rbf(fmaxf(__builtin_fmaf(-m, m, q * (1.f / D)), 0.f)) + 1e-5f)));
}
DI unsigned helper(int half, int hr, const u16* x, const u16* wqkv, const u16* wg, const u16* wu, const u16* wd, const u16* wo, const char* code, char* ws STAMP_ARGS) {
  const int tid = threadIdx.x, lane = tid & 63, w = __builtin_amdgcn_readfirstlane(tid >> 6);
  const int sub = lane >> 3, ch = lane & 7;
  unsigned tag;
  if (w >= 6) {
#pragma unroll 1
    for (int i = 0; i < CODE_KB / 4; ++i) dma16(SPARE + w * 2048, lane * 16, code + (4 * i + (hr & 1) * 2 + w - 6) * 1024);
  }
  sload_epoch(tag, ws);
  {
    u32x4 xr[2];
    const char* xp = (const char*)x + (size_t)(8 * blockIdx.x + w) * (D * 2) + min(lane, 39) * 32;
    xr[0] = *(const GAS u32x4*)xp; xr[1] = *(const GAS u32x4*)(xp + 16);
    float s, q;
    row_sums(xr, s, q);
    if (lane == 63) st_gran(ws + W_ROWST + (8 * blockIdx.x + w) * 8, tag + 1u, row_stat(s, q));
    wait_v<0>();
  }
  if (w < 6) {
    const int ng = 14 - half, n0 = hr + 8 * w, n = n0 < 15 ? n0 + 2 * ng : n0 - 15, m = n0 < 15 + 2 * ng ? n - 2 * ng : 15;
    if (m < 15) {
      const int sl = n < ng ? 14 * half + n : m < 0 ? 27 + 14 * half + n - ng
                   : m < 5 ? 54 + (half ? (m < 2 ? 3 + m : 5 + m) : (m < 3 ? m : 2 + m)) : m < 10 ? 59 + 5 * half + m : 64 + 5 * half + m;
      const char* base = sl < 27 ? (const char*)wg + sl * 128 : sl < 54 ? (const char*)wu + (sl - 27) * 128 : (const char*)wqkv + (sl - 54) * 128;
      const int pitch = sl < 54 ? F * 2 : 3 * D * 2;
      const unsigned reg = w * 24576, vo = sub * pitch + ch * 16;
      auto chunk = [&](int i) {
#pragma unroll
        for (int j = 0; j < 8; ++j) dma16(reg + (i % 3) * 8192 + j * 1024, vo, base + (size_t)((9 - i) * 64 + j * 8) * pitch);
      };
      chunk(0); chunk(1); chunk(2);
      const unsigned ra = reg + (8 * (lane >> 5) + ((lane >> 2) & 3)) * 128 + ((lane >> 4) & 1) * 32 + (lane & 3) * 8;
      const u32x4 ones = {0x3f803f80u, 0x3f803f80u, 0x3f803f80u, 0x3f803f80u};
      f32x16 c[2];
#pragma unroll
      for (int e = 0; e < 16; ++e) { c[0][e] = 0.f; c[1][e] = 0.f; }
#pragma unroll
      for (int i = 0; i < 10; ++i) {
        if (i < 8) wait_v<16>(); else if (i == 8) wait_v<8>(); else wait_v<0>();
#pragma unroll
        for (int ks = 0; ks < 4; ++ks)
#pragma unroll
          for (int t = 0; t < 2; ++t) { const unsigned a = ra + (i % 3) * 8192 + ks * 2048 + t * 64; c[t] = mfma32(ones, trfrag(a, a + 512), c[t]); }
        wait_vl<63>();
        if (i + 3 < 10) chunk(i + 3);
      }
      const float cs = lane >> 5 ? c[1][0] : c[0][0], hi = rbf(cs);
      st_gran(ws + W_COLST + (sl * 64 + lane) * 8, tag + 1u, pk2(hi, cs - hi));
    }
  }
  if (blockIdx.x == 255 && w == 7) {
#pragma unroll 1
    for (int spin = 0; spin < SPIN_MAX; ++spin) {
      __builtin_amdgcn_s_sleep(64);
      bool ok = true;
      u32x4 v[16];
#pragma unroll
      for (int i = 0; i < 16; ++i) v[i] = ld128s(mk_rsrc(ws), lane * 16, W_ROWST + i * 1024);
      SB();
#pragma unroll
      for (int i = 0; i < 16; ++i) ok &= (v[i][1] == tag + 1u) & (v[i][3] == tag + 1u);
      if (__all(ok)) break;
    }
    if (lane == 0) st_flag(ws + W_EPOCH, tag + 1u);
  }
  STAMP(1);
  wait_bar<0>();
#pragma unroll 1
  for (int i = 0; i < CODE_KB / 16; ++i) dma16(SPARE + w * 2048, lane * 16, code + (16 * i + (hr & 1) * 8 + w) * 1024);
  {
    const int b0 = ((half * 8 + hr) * 8 + w) * 24 * 1024;
#pragma unroll 1
    for (int i = 0; i < 24; ++i) {
      const int b = min(b0 + i * 1024, (F + D) * D * 2 - 1024);
      dma16(SPARE + w * 2048, lane * 16, b < F * D * 2 ? (const char*)wd + b : (const char*)wo + (b - F * D * 2));
    }
    wait_v<0>();
  }
  STAMP(2);
  return tag + 1u;
}

#define MSTAMP(i)
#define XSTAMP(i) STAMP(i)
#define KSTAMP(i)
#define OSTAMP(i)
#define P2STAMP(J)
#define XV(v) v
struct Frags { u32x4 x[4]; u32x4 w[2][3]; };
struct P1 {
  const char *xbase, *wbase, *xall, *wd;
  char* ws;
  unsigned voffx, voff, voff2, wdst, xdst, xa[2], wa[3], rowo, colo[2], tag, epoch;
  int isq, v0, wstep, w, cgp, rb, cb0, cbd, lc01, lc2;
  rsrc_t rsw;
  u32x2 gr[4], gc[2];
  u32x4 xrow[2];
  float rsum, rsq;
};
DI void w2_stage(unsigned dst, const char* src, int hs, int gs, int o, int lane, int go = -1) {
  const int rho = lane >> 3, pi = lane & 7;
  const unsigned v = (hs * (rho >> 2) + (rho & 3)) * (D * 2) + (((pi >> 1) ^ ((rho >> 1) & 1)) << 5) + (pi & 1) * 16;
  if (__builtin_constant_p(go)) dma16(dst + o * 1024, v ^ ((o & 1) << 6), src + o * gs * (D * 2));
  else dma16p(go, dst + o * 1024, v ^ ((o & 1) << 6), src + o * gs * (D * 2));
}
DI int rotv(const P1& c, int v) { const int r = v + c.v0; return r >= 10 ? r - 10 : r; }
DI int rotj(const P1& c, int j) { return 2 * rotv(c, j >> 1) + (j & 1); }
DI void issueX(const P1& c, int v, int half) { dma16(c.xdst + (v % 3) * 16384 + half * 8192, c.voffx, c.xbase + half * 64 * D * 2 + rotv(c, v) * 128); }
DI void issueW1(const P1& c, int j, int pr) { dma16(c.wdst + (j & 3) * 24576 + pr * 4096, c.voff, c.wbase + (size_t)rotj(c, j) * c.wstep + pr * 128); }
DI void ld_stat(P1& c, int e) {
  if (e < 4) c.gr[e] = ld64(c.rsw, c.rowo + e * 128);
  else if (e < 6) c.gc[e - 4] = ld64(c.rsw, c.colo[e - 4]);
}
template <int NT> DI void late_setup(P1& c) {
  const int lane = threadIdx.x & 63, sr = lane & 15, G = lane >> 4;
  c.rowo = W_ROWST + (128 * c.rb + 64 * ((threadIdx.x >> 7) & 1) + sr) * 8;
#pragma unroll
  for (int m = 0; m < 2; ++m) {
    const int z = min(4 * m + G, 5), sd = z >= 3, i = sd ? z - 3 : z;
    const int l1 = c.lc01, l2 = NT == 3 ? c.lc2 : l1, lc = i < 2 ? l1 + 16 * i : l2;
    c.colo[m] = W_COLST + (c.cb0 + (sd ? c.cbd : 0) + lc + sr) * 8;
  }
  asm volatile("" : "+v"(c.rowo), "+v"(c.colo[0]), "+v"(c.colo[1]));
}
template <int MODE> DI void mainloop(P1& c, f32x4 (&acc)[4][2][3] STAMP_ARGS) {
  constexpr int NT = MODE == 2 ? 2 : 3;
  auto readX = [&](Frags& f, int t, int st) { f.x[st] = ldsread128(c.xa[t & 1] + ((t >> 1) % 3) * 16384 + st * 2048); };
  auto readW = [&](Frags& f, int t, int n) {
    const unsigned a = c.wa[n % NT] + (t & 1) * 24576 + (n / NT) * 12288;
    f.w[n / NT][n % NT] = trfrag(a, a + 512);
  };
  auto issue = [&](int t, int e) {
    const int v = (t + 5) / 2, half = (t + 1) & 1, j = t + 4;
    if (MODE == 0) {
      if (e < 3) { if (j < 20) issueW1(c, j, e); }
      else if (e == 3) { if (v < 10) issueX(c, v, half); }
    } else if (MODE == 1) {
      if (e < 2 && v < 10) dma16(XS + (v % 3) * 16384 + half * 8192 + (2 * c.w + e) * 1024, c.voff2 ^ (e << 6), c.xbase + (half * 64 + e * 8) * D * 2 + rotv(c, v) * 128);
    } else {
      if (e < 6 && j < 20)
        dma16(WS + (j & 3) * 24576 + e * 4096 + (c.w - 4) * 1024, c.voff2, c.wbase + (size_t)rotj(c, j) * c.wstep + (e % 3) * 128 + (e / 3) * c.cbd * 2);
    }
  };
  auto step = [&](int t, const Frags& f, Frags& fn) {
    const bool rd = t + 1 < 20;
    if (rd && ((t + 1) & 1) == 0) {
#pragma unroll
      for (int i = 0; i < NT; ++i) { c.wa[i] = ((t + 1) & 2) ? c.wa[i] + 49152 : c.wa[i] - 49152; asm volatile("" : "+v"(c.wa[i])); }
    }
#pragma unroll
    for (int i = 0; i < 8 * NT; ++i) {
      const int n = i >> 2, st = (n & 1) ? 3 - (i & 3) : (i & 3);
      acc[st][n / NT][n % NT] = mfma16(f.w[n / NT][n % NT], f.x[st], acc[st][n / NT][n % NT]);
      if (MODE == 1) { if (i % 8 == 0) issue(t, i >> 3); } else if ((i & 1) == 0) issue(t, i >> 1);
      {
        const int lane = threadIdx.x & 63;
        if (t == 2 && i == 1) {
          asm volatile("" : "+v"(c.epoch), "+v"(c.xrow[0]), "+v"(c.xrow[1]));
          c.tag = __builtin_amdgcn_readfirstlane(c.epoch) + 1u;
          row_sums(c.xrow, c.rsum, c.rsq);
        }
        if (t == 2 && i == 15) {
          const u32x2 g = {row_stat(c.rsum, c.rsq), c.tag};
          __builtin_amdgcn_raw_buffer_store_b64(g, c.rsw, lane == 63 ? W_ROWST + (8 * blockIdx.x + (threadIdx.x >> 6)) * 8 : 0x80000000u, 0, 16);
        }
        if (t == 7 && i == 1) late_setup<NT>(c);
        if (t == 16 && i >= 3 && i < 9) ld_stat(c, i - 3);
        if (t == 18 && i == 0) asm volatile("" : "+v"(c.gr[0]), "+v"(c.gr[1]), "+v"(c.gr[2]), "+v"(c.gr[3]), "+v"(c.gc[0]), "+v"(c.gc[1]));
        if (QE && MODE != 0 && t >= 18 && (i & 1) && i < 8) {
          const int ks = 8 * (t - 18) + c.w;
          const bool split = ks % 3 == 2;
          w2_stage(EAR + c.w * 10240 + (t - 18) * 4096, c.wd + ks * 32 * (D * 2), split ? 4 : 16, split ? 8 : 4, i >> 1, lane, c.isq);
        }
        if (MODE == 0 && t >= 18 && (i & 1) && i < 8) {
          const int ks = 8 * (t - 18) + c.w;
          const bool split = ks % 3 == 2;
          w2_stage(EAR + c.w * 10240 + (t - 18) * 4096, c.wd + ks * 32 * (D * 2), split ? 4 : 16, split ? 8 : 4, i >> 1, lane);
        }
      }
      if (rd) {
        const int e = NT == 3 ? ((i & 1) ? i >> 1 : -1) : (i < 12 && i % 3 != 2 ? i - i / 3 : -1);
        if (e == 0) readW(fn, t + 1, 0);
        else if (e >= 1 && e <= 4) readX(fn, t + 1, e - 1);
        else if (e >= 5 && e < 4 + 2 * NT) readW(fn, t + 1, e - 4);
      }
      SB();
    }
  };
  Frags f0, f1;
  wait_bar<PV ? 11 : 15>();
  STAMP(1);
  if (PV == 1) { issueW1(c, 3, 0); issueW1(c, 3, 1); issueW1(c, 3, 2); issueX(c, 2, 0); SB(); }
#pragma unroll
  for (int i = 0; i < 4; ++i) readX(f0, 0, i);
#pragma unroll
  for (int i = 0; i < 2 * NT; ++i) readW(f0, 0, i);
  constexpr auto cnt = [](int t) {
    return (MODE == 0 ? (t <= 14 ? 4 : t == 15 ? 3 : 0) : MODE == 1 ? (t <= 14 ? 2 : 0) : (t <= 15 ? 6 : 0)) + (t == 2 ? 1 : t == 16 ? 6 : 0);
  };
#define STEP(T, FC, FN) wait_bar<cnt(T - 1) + cnt(T - 2)>(); step(T, FC, FN);
  wait_bar<12>(); step(0, f0, f1);
  wait_bar<7 + cnt(0)>(); step(1, f1, f0);
  STEP(2, f0, f1) STAMP(7); STEP(3, f1, f0) STEP(4, f0, f1)
  STEP(5, f1, f0) STEP(6, f0, f1) STEP(7, f1, f0) STEP(8, f0, f1) STEP(9, f1, f0)
  MSTAMP(8); STEP(10, f0, f1) STEP(11, f1, f0) STEP(12, f0, f1) MSTAMP(10); STEP(13, f1, f0) STEP(14, f0, f1)
  STEP(15, f1, f0) MSTAMP(11); STEP(16, f0, f1) MSTAMP(12);
  STEP(17, f1, f0)
  MSTAMP(13);
  wait_bar<6>();
  MSTAMP(15);
  step(18, f0, f1);
#undef STEP
  wait_vl<63>(); step(19, f1, f0);
}

struct W2 {
  const char *wd, *src7, *src8, *src9;
  int w, so7, gs7, gs8, gs9;
  unsigned ra[2][4];
  DI void init(const u16* wd_, int cgp) {
    w = __builtin_amdgcn_readfirstlane(threadIdx.x >> 6);
    wd = (const char*)wd_ + cgp * 128;
  }
  DI void init_o(const u16* wo_, int cgp) {
    const char* wo = (const char*)wo_ + cgp * 128;
    so7 = w < 4 ? 2 * w : 3 * w - 4;
    auto osrc = [&](int oi) { return wo + (oi >= 16 ? 160 * (oi - 16) + 16 : 40 * oi) * (D * 2); };
    auto ogs = [&](int oi) { return oi >= 16 ? 40 : 4; };
    src7 = osrc(so7); src8 = osrc(so7 + 1); src9 = osrc(so7 + 2);
    gs7 = ogs(so7); gs8 = ogs(so7 + 1); gs9 = ogs(so7 + 2);
    asm volatile("" : "+s"(src7), "+s"(src8), "+s"(src9), "+s"(so7), "+s"(gs7), "+s"(gs8), "+s"(gs9));
  }
  static DI int area(int s) { return (s >> 2) & 1 || s >= 8 ? ((s >> 1) & 1) == 0 : 0; }
  DI unsigned lds(int s) const { return area(s) ? LAT + w * 8192 + (s & 1) * 4096 : EAR + w * 10240 + (s & 1) * 4096; }
  DI int hks(int s) const { return s < 6 ? 8 * s + w : min(48 + w, 53); }
  DI void stage1(int s, int o, int lane, int go) const {
    const bool split = hks(s) % 3 == 2;
    if (s < 7) w2_stage(lds(s), wd + hks(s) * 32 * (D * 2), split ? 4 : 16, split ? 8 : 4, o, lane, go);
    else w2_stage(lds(s), s == 7 ? src7 : s == 8 ? src8 : src9, 20, s == 7 ? gs7 : s == 8 ? gs8 : gs9, o, lane, go);
  }
  DI void stage(int s, int go = -1) const {
    int lane = __builtin_amdgcn_mbcnt_hi(~0u, __builtin_amdgcn_mbcnt_lo(~0u, 0u));
    asm volatile("" : "+v"(lane));
    stage1(s, 0, lane, go); stage1(s, 1, lane, go); stage1(s, 2, lane, go); stage1(s, 3, lane, go);
  }
  DI u32x4 frag(int s, int n) const { const unsigned a = ra[area(s)][n] + (s & 1) * 4096; return trfrag(a, a + 512); }
  DI void frags(u32x4 (&d)[4], int s) const {
#pragma unroll
    for (int n = 0; n < 4; ++n) d[n] = frag(s, n);
  }
  DI void set_ra(int h, int lane) {
    const int G = lane >> 4;
#pragma unroll
    for (int i = 0; i < 4; ++i) {
      ra[h][i] = lds(4 * h) + ((G * 1024 + ((lane >> 2) & 3) * 128 + ((2 * (G & 1) | ((lane >> 3) & 1)) << 5) + (lane & 3) * 8) ^ (((i + w) & 3) << 5));
      asm volatile("" : "+v"(ra[h][i]));
    }
  }
  DI int aslot(int s) const { return s < 7 ? hks(s) : 74 + so7 + s - 7; }
};

template <bool GLU> DI unsigned epilogue1(P1& c, f32x4 (&acc)[4][2][3], W2& W, u32x4 (&wr)[3][4], int cg, char* ws STAMP_ARGS) {
  int tid = threadIdx.x;
  asm volatile("" : "+v"(tid));
  const int lane = tid & 63, w = __builtin_amdgcn_readfirstlane(tid >> 6), sr = lane & 15, G = lane >> 4;
  const int wn = ((w >> 2) << 1) | (w & 1), wm = (w >> 1) & 1, rb = c.rb, lc01 = c.lc01, lc2 = c.lc2;
  constexpr bool glu = GLU;
  const bool isq = cg >= 13, nt3 = glu || w < 4;
  const unsigned tag = c.tag;
  auto bounce = [&]() {
    W.set_ra(0, lane);
    W.frags(wr[0], 0); W.frags(wr[1], 1);
    wait_vl<63>();
    W.stage(2); W.stage(3);
    SB();
  };
  auto stats_ok = [&]() { return __all(c.gr[0].y == tag && c.gr[1].y == tag && c.gr[2].y == tag && c.gr[3].y == tag && c.gc[0].y == tag && c.gc[1].y == tag); };
  if (!stats_ok()) {
#pragma unroll 1
    for (int spin = 0; spin < SPIN_MAX; ++spin) {
#pragma unroll
      for (int e = 0; e < 6; ++e) ld_stat(c, e);
      asm volatile("" : "+v"(c.gr[0]), "+v"(c.gr[1]), "+v"(c.gr[2]), "+v"(c.gr[3]), "+v"(c.gc[0]), "+v"(c.gc[1]));
      if (stats_ok()) break;
      __builtin_amdgcn_s_sleep(2);
    }
  }
  unsigned cs0 = c.gc[0].x, cs1 = c.gc[1].x;
  asm volatile("" : "+v"(cs0), "+v"(cs1));
  float sdv[4], rs[4], rsl[4], ce[4];
#pragma unroll
  for (int st = 0; st < 4; ++st) {
    const unsigned g = c.gr[st].x;
    sdv[st] = hi16(g); rs[st] = __builtin_amdgcn_rcpf(sdv[st]);
    rsl[st] = 1.44269504f * rs[st]; ce[st] = 1e-6f * sdv[st];
    const unsigned mm = (g & 0xffffu) * 0x10001u;
#pragma unroll
    for (int z = 0; z < 6; ++z)
      acc[st][z / 3][z % 3] = mfma16k16(z < 4 ? cs0 : cs1, G == (z & 3) ? mm : 0u, acc[st][z / 3][z % 3]);
  }
  if (!GLU) { SB(); asm volatile("s_nop 7"); SB(); }
  STAMP(9);
  const rsrc_t rsw = c.rsw;
  const unsigned actb = W_ACT + (8 * rb + 4 * wm) * NSL * 1024 + lane * 16;
  const unsigned acts = W_ACT + (8 * rb + 4 * wm + (G & 1)) * NSL * 1024 + ((2 * (wn & 1) + (G >> 1)) * 16 + sr) * 16;
  auto store_split = [&](int pair, int ks, u32x2 a, u32x2 b) {
    const auto r0 = __builtin_amdgcn_permlane16_swap(a.x, b.x, false, false), r1 = __builtin_amdgcn_permlane16_swap(a.y, b.y, false, false);
    st128(rsw, acts + (2 * pair * NSL + ks) * 1024, u32x4{r0[0], r1[0], r0[1], r1[1]});
  };
  auto phi4 = [&](int st, f32x4 v) {
    return u32x2{pk2(phi(v[0], rs[st], rsl[st], ce[st]), phi(v[1], rs[st], rsl[st], ce[st])), pk2(phi(v[2], rs[st], rsl[st], ce[st]), phi(v[3], rs[st], rsl[st], ce[st]))};
  };
  auto glu4 = [&](int st, int i) {
    float h[4];
#pragma unroll
    for (int j = 0; j < 4; ++j) {
      const float e = __builtin_amdgcn_exp2f(acc[st][0][i][j] * -rsl[st]);
      h[j] = acc[st][1][i][j] * __builtin_amdgcn_rcpf(__builtin_fmaf(e, sdv[st], sdv[st]));
    }
    return u32x2{pk2(h[0], h[1]), pk2(h[2], h[3])};
  };
  if (glu) {
#pragma unroll
    for (int pair = 0; pair < 2; ++pair) {
#pragma unroll
      for (int st = 2 * pair; st < 2 * pair + 2; ++st) {
        const u32x2 a = glu4(st, 0), b = glu4(st, 1);
        st128(rsw, actb + (st * NSL + 6 * cg + (lc01 >> 5)) * 1024, u32x4{a.x, a.y, b.x, b.y});
      }
      store_split(pair, 6 * cg + (lc2 >> 5), glu4(2 * pair, 2), glu4(2 * pair + 1, 2));
    }
  } else if (isq) {
    const int sp0 = 54 + 5 * (cg - 13);
#pragma unroll
    for (int sd = 0; sd < 2; ++sd)
#pragma unroll
      for (int pair = 0; pair < 2; ++pair) {
#pragma unroll
        for (int st = 2 * pair; st < 2 * pair + 2; ++st) {
          const u32x2 a = phi4(st, acc[st][sd][0]), b = phi4(st, acc[st][sd][1]);
          st128(rsw, actb + (st * NSL + sp0 + 10 * sd + (lc01 >> 5)) * 1024, u32x4{a.x, a.y, b.x, b.y});
        }
        if (nt3) store_split(pair, sp0 + 10 * sd + 2, phi4(2 * pair, acc[2 * pair][sd][2]), phi4(2 * pair + 1, acc[2 * pair + 1][sd][2]));
      }
  } else {
    auto swz = [](int r) { return (r & 5) | ((r & 2) << 2) | ((r & 8) >> 2); };
    unsigned ia[3][2];
#pragma unroll
    for (int i = 0; i < 3; ++i) {
      const int u = ((i < 2 ? lc01 + 16 * i : lc2) >> 2) + G;
      ia[i][0] = KVI + (64 * wm + sr) * 640 + ((u ^ swz(sr)) << 3); ia[i][1] = KVI + (64 * wm + sr) * 640 + (((u + 40) ^ swz(sr)) << 3);
    }
#pragma unroll
    for (int i = 0; i < 3; ++i) {
      if (i == 2 && !nt3) break;
#pragma unroll
      for (int st = 0; st < 4; ++st) {
        const f32x4 v = acc[st][1][i];
        *(LAS u32x2*)(ia[i][0] + st * 10240) = phi4(st, acc[st][0][i]);
        *(LAS u32x2*)(ia[i][1] + st * 10240) = u32x2{pk2(v[0] * rs[st], v[1] * rs[st]), pk2(v[2] * rs[st], v[3] * rs[st])};
      }
    }
    KSTAMP(8);
    wait_bar<63>();
    KSTAMP(10);
    const int hf = lane >> 5, q = (lane >> 2) & 3, p = lane & 3;
    const int uk = min(5 * w + 4 * (G & 1) + p, 39);
    unsigned rk[2], rv[2];
#pragma unroll
    for (int t = 0; t < 2; ++t) {
      const int row = 8 * hf + 4 * t + q;
      rk[t] = KVI + row * 640 + ((uk ^ swz(row)) << 3); rv[t] = KVI + row * 640 + (((uk + 40) ^ swz(row)) << 3);
    }
    u32x4 kf[8], vf[8];
#pragma unroll
    for (int ks = 0; ks < 8; ++ks) {
      const unsigned hi = ks >= 4 ? 40960 : 0;
      kf[ks] = trfrag(rk[0] + hi + (ks & 3) * 10240, rk[1] + hi + (ks & 3) * 10240);
      vf[ks] = trfrag(rv[0] + hi + (ks & 3) * 10240, rv[1] + hi + (ks & 3) * 10240);
    }
    SB();
    f32x16 kv, kz;
#pragma unroll
    for (int i = 0; i < 16; ++i) { kv[i] = 0.f; kz[i] = 0.f; }
    const bool one = (lane & 31) >= 20;
#pragma unroll
    for (int ks = 0; ks < 8; ++ks) {
#pragma unroll
      for (int i = 0; i < 4; ++i) vf[ks][i] = one ? 0x3f803f80u : vf[ks][i];
      if (ks & 1) kz = mfma32(kf[ks], vf[ks], kz); else kv = mfma32(kf[ks], vf[ks], kv);
    }
    kv += kz;
    KSTAMP(11);
    const unsigned sc = SPARE + w * 2048;
#pragma unroll
    for (int j = 0; j < 3; ++j)
      *(LAS f32x4*)(((lane & 31) <= 20 && (j < 2 || hf == 0)) ? sc + ((lane & 31) * 5 + 2 * j + hf) * 16 : sc + 2032) = f32x4{kv[4 * j], kv[4 * j + 1], kv[4 * j + 2], kv[4 * j + 3]};
    const unsigned po = W_KVP + (rb * 16 + 4 * (cg - 9) + (w >> 1)) * 4096 + (w & 1) * 1792 + lane * 16;
    st128(rsw, po, ldsread128(sc + lane * 16));
    st128(rsw, lane < 48 ? po + 1024 : 0x80000000u, ldsread128(sc + 1024 + lane * 16));
  }
  STAMP(3);
  if (!glu && !isq) {
    asm volatile("s_waitcnt vmcnt(0)" ::: "memory");
    st_flags(rsw, W_FLAGKV + ((8 * (cg - 9) + w) * 16 + rb) * FL, 1, lane, tag);
  } else {
    if (glu && (EB == 2 || (EB == 1 && w < 4))) { wait_v<6>(); bounce(); }
    asm volatile("s_waitcnt vmcnt(0)" ::: "memory");
  }
  if (glu && EB == 0 && w < 4) bounce();
  wait_bar<63>();
  if (w == 0) st_flags(rsw, W_FLAG1 + (rb * 16 + cg) * FL, 1, lane, tag);
  if (glu && EB < 2 && w >= 4) bounce();
  return tag;
}

template <bool GLU> DI unsigned phase1(int rb, int cg, int cgp, W2& W, u32x4 (&wr)[3][4], const u16* x, const u16* wqkv, const u16* wg, const u16* wu, const u16* wd, char* ws STAMP_ARGS) {
  const int tid = threadIdx.x, lane = tid & 63, w = __builtin_amdgcn_readfirstlane(tid >> 6);
  const int wn = ((w >> 2) << 1) | (w & 1), wm = (w >> 1) & 1;
  P1 c;
  c.v0 = KROT ? (5 * (rb >> 2)) >> 1 : 0;
  const int side = w & 1, oct = w >> 1, rho = lane >> 3, pi = lane & 7;
  c.voffx = (8 * w + rho) * (D * 2) + ((pi ^ (4 * (w & 1) + (rho >> 1))) << 4);
  c.xbase = (const char*)x + (size_t)rb * 128 * D * 2;
  c.wdst = WS + (side * 12 + oct) * 1024; c.xdst = XS + w * 1024;
  issueX(c, 0, 0); issueX(c, 0, 1);
  constexpr bool glu = GLU;
  const bool isq = cg >= 13;
  auto col0 = [&](int sd) { return glu ? 192 * cg : isq ? 160 * (cg - 13) + 320 * sd : 640 + 640 * sd + 160 * (cg - 9); };
  const int shift = (!glu && !(cg & 1)) ? 32 : 0;
  c.wbase = glu ? (side ? (const char*)wu : (const char*)wg) : (const char*)wqkv;
  const int pitch = glu ? F * 2 : 3 * D * 2;
  c.voff = (col0(side) - shift + 16 * ((pi >> 1) ^ (2 * (oct & 1) | ((rho >> 1) & 1)))) * 2 + (8 * oct + rho) * pitch + (pi & 1) * 16;
  c.wstep = 32 * pitch;
  issueW1(c, 0, 0); issueW1(c, 0, 1); issueW1(c, 0, 2);
  STAMP(5);
  SB(); __builtin_amdgcn_s_barrier(); SB();
  issueW1(c, 1, 0); issueW1(c, 1, 1); issueW1(c, 1, 2); issueX(c, 1, 0);
  SB();
  const int sr = lane & 15, G = lane >> 4;
  const int lc01 = 32 * (3 * (wn >> 1) + (wn & 1)), lc2 = 32 * (3 * (wn >> 1) + 2) + 16 * (wn & 1);
  c.xa[0] = XS + (64 * wm + sr) * 128 + ((G ^ (sr >> 1)) << 4); c.xa[1] = c.xa[0] ^ 64;
  {
    const int q = (lane >> 2) & 3, p = lane & 3;
    const unsigned b0 = WS + G * 1024 + q * 128 + p * 8, sw = 2 * (G & 1) | (q >> 1);
#pragma unroll
    for (int i = 0; i < 3; ++i) { const int lc = (i < 2 ? lc01 + 16 * i : lc2) + shift; c.wa[i] = b0 + (lc >> 6) * 4096 + ((((lc >> 4) & 3) ^ sw) << 5); }
  }
  asm volatile("" : "+v"(c.xa[0]), "+v"(c.xa[1]), "+v"(c.wa[0]), "+v"(c.wa[1]), "+v"(c.wa[2]));
  SB();
  issueW1(c, 2, 0); issueW1(c, 2, 1); issueW1(c, 2, 2); issueX(c, 1, 1);
  if (PV == 0) { issueW1(c, 3, 0); issueW1(c, 3, 1); issueW1(c, 3, 2); issueX(c, 2, 0); }
  {
    const char* xr = (const char*)x + (size_t)(8 * blockIdx.x + w) * (D * 2);
    gload<0>(c.xrow[0], min(lane, 39) * 32, xr); gload<16>(c.xrow[1], min(lane, 39) * 32, xr);
    gload32(c.epoch, W_EPOCH, ws);
  }
  SB();
  c.rsw = mk_rsrc(ws);
  c.ws = ws; c.rb = rb; c.lc01 = lc01; c.lc2 = lc2; c.w = w; c.isq = max(cg - 12, 0);
  c.cb0 = col0(0) + (glu ? 0 : 2 * F); c.cbd = glu ? F : isq ? 320 : 640;
  c.wd = (const char*)wd + cgp * 128;
  f32x4 acc[4][2][3];
#pragma unroll
  for (int i = 0; i < 24; ++i) acc[i / 6][(i / 3) & 1][i % 3] = f32x4{0.f, 0.f, 0.f, 0.f};
  if (P1PRIO && w >= 4) __builtin_amdgcn_s_setprio(1);
  if (GLU) mainloop<0>(c, acc STAMP_PASS);
  else if (w < 4) {
    c.voff2 = (16 * w + rho) * (D * 2) + ((pi ^ (rho >> 1)) << 4);
    mainloop<1>(c, acc STAMP_PASS);
  } else {
    c.voff2 = (col0(0) - shift + 16 * ((pi >> 1) ^ (2 * (w & 1) | ((rho >> 1) & 1)))) * 2 + (8 * (w - 4) + rho) * pitch + (pi & 1) * 16;
    mainloop<2>(c, acc STAMP_PASS);
  }
  STAMP(2);
  return epilogue1<GLU>(c, acc, W, wr, cg, ws STAMP_PASS);
}

template <class F1> DI void o_share(int P, int rbo, int nrb, unsigned fo, unsigned tag, char* ws, int lane, F1 w1 STAMP_ARGS) {
  const int w = __builtin_amdgcn_readfirstlane(threadIdx.x >> 6), sr = lane & 15, G = lane >> 4;
  const rsrc_t rsw = mk_rsrc(ws);
  const unsigned l16 = lane * 16;
  if (w == 0) poll(lane < 32 ? ws + W_FLAGKV + (P * 32 + lane) * FL : ws + W_FLAG1 + ((rbo + lane - 32) * 16 + 13 + ((P >> 2) & 1)) * FL,
                   lane < 32 + nrb, tag);
  else if (!LEAN) w1();
  const int s0 = (40 * P) / 32;
  unsigned ta[8][2];
#pragma unroll
  for (int i = 0; i < 8; ++i) {
    const int b = i >> 2, sp = s0 + ((i >> 1) & 1);
    const int e = (i & 1) == 0 ? sr : (sr & 3) == b ? 20 : ((sr & 2) && (sr >> 3) == b) ? 16 + 2 * ((sr >> 2) & 1) + (sr & 1) : -1;
#pragma unroll
    for (int hl = 0; hl < 2; ++hl) {
      const int d = 32 * sp + (sp % 5 == 2 ? 8 * G + 4 * hl : 16 * hl + 4 * G) - 20 * (2 * P + b);
      ta[i][hl] = (e >= 0 && d >= 0 && d < 20) ? T2 + b * 896 + e * 40 + d * 2 : T2 + 1792;
      asm volatile("" : "+v"(ta[i][hl]));
    }
  }
  if (w == 7) *(LAS u32x2*)(T2 + 1792) = u32x2{0u, 0u};
  wait_bar<63>();
  XSTAMP(10);
  const unsigned tile = W_ACT + (8 * rbo + w) * NSL * 1024;
  u32x4 pqf[3][2], part[16];
  const bool pact = w >= 4 && (w - 4) * 64 + lane < 224;
  if (pact) {
#pragma unroll
    for (int i = 0; i < 16; ++i) part[i] = ld128s(rsw, l16, W_KVP + (i * 16 + P) * 4096 + (w - 4) * 1024);
  }
#pragma unroll
  for (int r = 0; r < 3; ++r)
#pragma unroll
    for (int i = 0; i < 2; ++i) pqf[r][i] = ld128s(rsw, r < 2 || nrb == 3 ? l16 : 0x80000000u, tile + (r * 8 * NSL + 54 + s0 + i) * 1024);
  const unsigned hf = ld32(rsw, W_FLAG1 + fo);
  SB();
  if (pact) {
    f32x4 t[4];
#pragma unroll
    for (int i = 0; i < 4; ++i)
      t[i] = (__builtin_bit_cast(f32x4, part[4 * i]) + __builtin_bit_cast(f32x4, part[4 * i + 1])) + (__builtin_bit_cast(f32x4, part[4 * i + 2]) + __builtin_bit_cast(f32x4, part[4 * i + 3]));
    t[0] = (t[0] + t[1]) + (t[2] + t[3]);
    *(LAS u32x2*)(T2 + ((w - 4) * 64 + lane) * 8) = u32x2{pk2(t[0][0], t[0][1]), pk2(t[0][2], t[0][3])};
  }
  OSTAMP(2);
  wait_bar<63>();
  OSTAMP(3);
  u32x4 a[8];
#pragma unroll
  for (int i = 0; i < 8; ++i) { const u32x2 lo = *(LAS u32x2*)ta[i][0], hi = *(LAS u32x2*)ta[i][1]; a[i] = u32x4{lo.x, lo.y, hi.x, hi.y}; }
#pragma unroll
  for (int r = 0; r < 3; ++r) {
    const f32x4 z = {0.f, 0.f, 0.f, 0.f};
    const f32x4 na = mfma16(a[2], pqf[r][1], mfma16(a[0], pqf[r][0], z)), nb = mfma16(a[6], pqf[r][1], mfma16(a[4], pqf[r][0], z));
    const f32x4 xx = mfma16(a[7], pqf[r][1], mfma16(a[5], pqf[r][0], mfma16(a[3], pqf[r][1], mfma16(a[1], pqf[r][0], z))));
    const float ra = __builtin_amdgcn_rcpf(xx[0] + 1e-6f), rb = __builtin_amdgcn_rcpf(xx[1] + 1e-6f), rl = G < 2 ? ra : rb;
    const unsigned tr = (r < 2 || nrb == 3) ? tile + r * 8 * NSL * 1024 : 0x80000000u;
    st128(rsw, tr + (74 + P) * 1024 + l16, u32x4{pk2(na[0] * ra, na[1] * ra), pk2(na[2] * ra, na[3] * ra), pk2(nb[0] * rb, nb[1] * rb), pk2(nb[2] * rb, nb[3] * rb)});
    st32(rsw, tr + (90 + (P >> 2)) * 1024 + ((P & 3) * 16 + sr) * 16 + 4 * G, pk2(xx[2] * rl, xx[3] * rl));
  }
  OSTAMP(5);
  asm volatile("s_waitcnt vmcnt(0)" ::: "memory");
  st_flags(rsw, W_FLAGO + ((8 * (rbo + (lane >> 3)) + w) * 16 + P) * FL, nrb, lane, tag);
  SB();
  if (w == 0 || LEAN) w1();
  if (!__all(sr >= 9 || hf == tag)) poll(ws + W_FLAG1 + fo, sr < 9, tag);
}

template <int NS>
DI void tail(W2& W, u32x4 (&wr)[3][4], int st0, int cgp, int P, int jj, int pre, unsigned tag, const u16* x, const u16* wo, u16* out, char* ws STAMP_ARGS) {
  int lane = __builtin_amdgcn_mbcnt_hi(~0u, __builtin_amdgcn_mbcnt_lo(~0u, 0u));
  asm volatile("" : "+v"(lane));
  const int w = W.w, sr = lane & 15;
  constexpr int RW = NS == 6 ? 3 : 2;
  W.init_o(wo, cgp);
  W.set_ra(0, lane); W.set_ra(1, lane);
  const rsrc_t rsw = mk_rsrc(ws);
  const unsigned l16 = lane * 16;
  const unsigned fo = (((lane & 16) ? (st0 + NS - 1) >> 3 : st0 >> 3) * 16 + min(sr, 8)) * FL;
  const unsigned mail = 2032 + (w == 0 ? (lane & 7) : w) * 2048;
  if (w == 0) *(LAS unsigned*)mail = ~tag;
  const int rot = w >> 2;
  auto gst = [&](int st) { return st == NS - 1 ? (rot ? 0 : NS - 1) : st + rot; };
  const unsigned abase = W_ACT + st0 * NSL * 1024;
  constexpr int AH = NS == 6 ? 2 : 5;
  u32x4 a[10][NS], wl[4];
  auto issueA1 = [&](int s, int st) { a[s][st] = ld128s(rsw, l16, abase + (W.aslot(s) + gst(st) * NSL) * 1024); };
  auto first = [&]() {
#pragma unroll
    for (int e = 0; e < AH * NS; ++e) issueA1(e / NS, e % NS);
    SB();
  };
  if (NS == 6) {
    W.stage(4); W.stage(5);
    wait_v<8>();
    W.frags(wr[2], 2);
    wait_vl<63>();
    W.stage(6);
    XSTAMP(8);
    if (w == 0) poll(ws + W_FLAG1 + fo, sr < 9, tag);
    wait_bar<63>();
  } else {
    auto w1 = [&]() { if (pre != 2) { W.stage(0); W.stage(1); } W.stage(4); W.stage(5); SB(); };
    auto bounce = [&]() {
      wait_v<8>();
      W.frags(wr[0], 0); W.frags(wr[1], 1);
      wait_vl<63>();
      W.stage(2); W.stage(3);
      SB();
    };
    if (pre) { w1(); bounce(); }
    o_share(P, NS == 3 ? 3 * jj : 4 + 2 * jj, NS == 3 ? 3 : 2, fo, tag, ws, lane, [&]() { if (!pre) w1(); } STAMP_PASS);
    XSTAMP(8);
    if (!pre) bounce();
  }
  first();
  XSTAMP(11);
  __builtin_amdgcn_s_setprio(3);
  f32x4 acc[NS][4];
  u32x2 oflag;
  const unsigned oo = w == 0 ? W_FLAGO + (st0 * 16 + min(lane, 16 * NS - 1)) * FL : 0x80000000u, oo2 = w == 0 ? W_FLAGO + (st0 * 16 + min(64 + lane, 16 * NS - 1)) * FL : 0x80000000u;
  auto vm = [&](int s, int e) {
    if (RW == 2 && s == 3 && e == 0) W.stage(6);
    if (s == 4 && e == 0) W.stage(7);
    if (s + AH <= 6 && e < NS) issueA1(s + AH, e);
    if (s == 4 && e == NS) { oflag.x = ld32(rsw, oo); oflag.y = ld32(rsw, oo2); }
    if (s == 5 && e == 0) W.stage(8);
  };
  auto pslot = [&](int src, int l) { const int k = l - 1 - (NS > 3 && l > 8) - (NS == 6 && l > 16); return k < 10 ? EAR + src * 10240 + k * 1024 : k < 18 ? LAT + src * 8192 + (k - 10) * 1024 : src * 2048 + (k - 18) * 1024; };
  auto put = [&](int j) {
    const int pn = j / NS, pst = (pn & 1) ? NS - 1 - j % NS : j % NS, l = 4 * pst + pn;
    if (l != 0 && (l != 8 || NS == 3) && (l != 16 || NS != 6) && l < 22) *(LAS f32x4*)(pslot(w, l) + l16) = acc[pst][pn];
  };
  auto slot = [&](int s, const u32x4 (&wc)[4], bool last) {
    if (s == 2) __builtin_amdgcn_s_setprio(2);
    if (s == 4) __builtin_amdgcn_s_setprio(1);
    if (s == 7) __builtin_amdgcn_s_setprio(0);
#pragma unroll
    for (int i = 0; i < 4 * NS; ++i) {
      const int n = i / NS, st = (n & 1) ? NS - 1 - i % NS : i % NS;
      if (s == 0) mfma16_new(acc[st][n], wc[n], a[s][st]); else mfma16_acc(acc[st][n], wc[n], a[s][st]);
      if ((i & 1) == 0) vm(s, i >> 1);
      if (s >= RW - 1 && s <= 7 && s != 6 && i % NS == NS - 1) wl[n] = W.frag(s + 1, n);
      if (last && i >= 8) put(i - 8);
      SB();
    }
    if (last) {
      asm volatile("s_nop 7");
      SB();
#pragma unroll
      for (int j = 4 * NS - 8; j < 4 * NS; ++j) put(j);
    }
  };
  P2STAMP(0); slot(0, wr[0], false);
  P2STAMP(1); slot(1, wr[1], false);
  P2STAMP(2); slot(2, RW == 3 ? wr[2] : wl, false);
  P2STAMP(3); slot(3, wl, false);
  P2STAMP(4); slot(4, wl, false);
  if (RW == 2) wait_v<6>();
  slot(5, wl, false);
  if (!__all(oflag.x == tag && oflag.y == tag)) {
#pragma unroll 1
    for (int spin = 0; spin < SPIN_MAX; ++spin) {
      u32x2 f;
      if (w == 0) { f.x = ld32(rsw, oo); f.y = ld32(rsw, oo2); } else f.x = f.y = *(volatile LAS unsigned*)mail;
      if (__all(f.x == tag && f.y == tag)) break;
      __builtin_amdgcn_s_sleep(POLL_SLEEP);
    }
    __builtin_amdgcn_fence(__ATOMIC_ACQUIRE, "wavefront");
  }
  if (w == 0) *(volatile LAS unsigned*)mail = tag;
  SB();
  XSTAMP(15);
#pragma unroll
  for (int e = 0; e < 2 * NS; ++e) issueA1(7 + e / NS, e % NS);
  int tz = __builtin_amdgcn_mbcnt_hi(~0u, __builtin_amdgcn_mbcnt_lo(~0u, 0u));
  asm volatile("" : "+v"(tz));
  const unsigned eo = (16 * (st0 + rot) + (tz & 15)) * D + 64 * cgp + 16 * (w & 3) + 4 * (tz >> 4);
  constexpr int NI = (NS + 1) / 2;
  u32x2 xres[NI];
#pragma unroll
  for (int i = 0; i < NI; ++i) xres[i] = *(const GAS u32x2*)(x + (i == NI - 1 && (NS & 1) && rot ? 0 : 32 * i * D) + eo);
  SB();
  u32x4 wx[4];
  W.frags(wx, 7);
  SB();
  if (w < 6) slot(6, wl, false);
  if (rot) {
    W.stage(9);
#pragma unroll
    for (int e = 0; e < NS; ++e) issueA1(9, e);
  }
  XSTAMP(12);
  slot(7, wx, false);
  if (rot) {
    wait_v<0>();
    W.frags(wx, 9);
    slot(9, wx, false);
  }
  slot(8, wl, true);
  XSTAMP(13);
  wait_bar<63>();
  OSTAMP(7);
  auto finish = [&](int i, f32x4 s) {
    const u32x2 o = {pk2(s[0] + lo16(xres[i].x), s[1] + hi16(xres[i].x)), pk2(s[2] + lo16(xres[i].y), s[3] + hi16(xres[i].y))};
    *(GAS u32x2*)(out + 32 * i * D + eo) = o;
  };
  f32x4 v[NI][8];
#pragma unroll
  for (int j = 1; j < 8; ++j) {
    const int dh = j >> 2, dn = j & 3, sw = 4 * (rot ^ dh) + ((w - dn) & 3);
#pragma unroll
    for (int i = 0; i < NI; ++i) {
      const int l0 = 4 * (dh ? (2 * i + NS - 1) % NS : 2 * i) + dn, l1 = 4 * (dh ? (2 * i + 1) % NS : 2 * i) + dn;
      const int a0 = pslot(sw, min(l0, 21)), a1 = pslot(sw, min(l1, 21));
      v[i][j] = *(LAS f32x4*)((rot ? a1 : a0) + l16);
      if (l0 >= 22 || l1 >= 22) v[i][j] *= (rot ? l1 : l0) >= 22 ? 0.f : 1.f;
    }
  }
  f32x4 sum[NI];
#pragma unroll
  for (int i = 0; i < NI; ++i) {
    sum[i] = ((acc[2 * i][0] + v[i][1]) + (v[i][2] + v[i][3])) + ((v[i][4] + v[i][5]) + (v[i][6] + v[i][7]));
    if (NS == 6 ? i == 1 : (i < NI - 1 || !(NS & 1) || !rot)) finish(i, sum[i]);
  }
  if constexpr (NS == 6) {
    wait_bar<63>();
    *(LAS f32x4*)((w * 2) * 1024 + l16) = acc[5][2];
    *(LAS f32x4*)((w * 2 + 1) * 1024 + l16) = acc[5][3];
    wait_bar<63>();
    const int o4 = rot ? 0 : 4;
    const f32x4 e = *(LAS f32x4*)((((o4 + ((w - 2) & 3)) * 2)) * 1024 + l16) + *(LAS f32x4*)(((o4 + ((w - 3) & 3)) * 2 + 1) * 1024 + l16);
    if (rot) sum[2] += e; else sum[0] += e;
    finish(0, sum[0]); finish(2, sum[2]);
  }
}

__global__ __launch_bounds__(512, 2) void fused(const u16* __restrict__ x, const u16* __restrict__ wqkv, const u16* __restrict__ wg,
                                                const u16* __restrict__ wu, const u16* __restrict__ wd, const u16* __restrict__ wo,
                                                u16* __restrict__ out, char* __restrict__ ws) {
  asm volatile("" : "+s"(x), "+s"(wqkv), "+s"(wg), "+s"(wu), "+s"(wd), "+s"(wo), "+s"(out), "+s"(ws));
  u64 pc0;
  asm volatile("s_getpc_b64 %0" : "=s"(pc0));
  const char* code = (const char*)(pc0 & ~255ull);
  const int xcd = blockIdx.x & 7, idx = blockIdx.x >> 3;
  STAMP_DECL
  STAMP(0);
  unsigned tag;
  const int half = xcd & 1;
  const int i7 = idx / 7, j7 = idx - 7 * i7;
  const int cg = idx >= 28 ? 4 : j7 < 4 ? 5 * half + j7 : j7 < 6 ? 5 + 2 * half + j7 : 13 + half;
  const int rb = 4 * (xcd >> 1) + (idx >= 28 ? 2 * half + idx - 28 : i7);
  const int lr = idx >= 30 ? idx : idx >= 28 ? idx - 12 : j7 < 4 ? 4 * i7 + j7 : j7 < 6 ? 14 + 2 * i7 + j7 : 26 + i7;
  const int xq = xcd & 3, band = 64 * (xcd >> 2);
  if (lr < 18 || lr >= 30) {
    const int r = lr < 18 ? lr : lr - 12, hi = r >= 10, st0 = band + 6 * (2 * xq + hi), cgp = r - 10 * hi;
    W2 W;
    u32x4 wr[3][4];
    W.init(wd, cgp);
    if (lr >= 30) {
      tag = helper(half, 2 * (xcd >> 1) + idx - 30, x, wqkv, wg, wu, wd, wo, code, ws STAMP_PASS);
      wait_bar<63>();
      W.stage(0); W.stage(1);
      W.set_ra(0, threadIdx.x & 63);
      wait_v<0>();
      W.frags(wr[0], 0); W.frags(wr[1], 1);
      wait_vl<63>();
      W.stage(2); W.stage(3);
    } else tag = phase1<true>(rb, cg, cgp, W, wr, x, wqkv, wg, wu, wd, ws STAMP_PASS);
    STAMP(4);
    tail<6>(W, wr, st0, cgp, 0, 0, 2, tag, x, wo, out, ws STAMP_PASS);
  } else {
    const int r = lr - 26;
    const int st0 = band + (lr < 26 ? 52 + 3 * xq : r < 2 ? 48 : 48 + 4 * xq), cgp = lr < 26 ? lr - 18 : r < 2 ? 2 * xq + r : 6 + r;
    W2 W;
    u32x4 wr[3][4];
    W.init(wd, cgp);
    tag = phase1<false>(rb, cg, cgp, W, wr, x, wqkv, wg, wu, wd, ws STAMP_PASS);
    STAMP(4);
    const int P = 2 * xcd + (lr & 1), jj = (lr - 18) >> 1;
    if (lr >= 26) tail<4>(W, wr, st0, cgp, P, jj, QE ? 2 : QP, tag, x, wo, out, ws STAMP_PASS);
    else tail<3>(W, wr, st0, cgp, P, jj, 0, tag, x, wo, out, ws STAMP_PASS);
  }
  STAMP(14);
  STAMP_OUT
}

extern "C" void kernel_launch(void* const* d_in, const int* in_sizes, int n_in, void* d_out, int out_size, void* d_ws, size_t ws_size,
                              hipStream_t stream) {
  const u16* x = (const u16*)d_in[0]; const u16* wqkv = (const u16*)d_in[1]; const u16* wo = (const u16*)d_in[2];
  const u16* wg = (const u16*)d_in[4]; const u16* wu = (const u16*)d_in[5]; const u16* wd = (const u16*)d_in[6];
  (void)hipFuncSetAttribute((const void*)fused, hipFuncAttributeMaxDynamicSharedMemorySize, LDS_BYTES);
  fused<<<256, 512, LDS_BYTES, stream>>>(x, wqkv, wg, wu, wd, wo, (u16*)d_out, (char*)d_ws);
}
```
